```python
import torch

def _softmax(x, axis=-1):
    return torch.softmax(x, dim=axis)
def _gelu(x):
    return torch.nn.functional.gelu(x, approximate="tanh")
def _silu(x):
    return torch.nn.functional.silu(x)
def _sigmoid(x):
    return torch.sigmoid(x)
def _layernorm(x, axis=-1, eps=1e-5):
    m = x.mean(dim=axis, keepdim=True)
    v = x.var(dim=axis, keepdim=True, unbiased=False)
    return (x - m) / torch.sqrt(v + eps)
def _scatter_add(tgt, idx, src, axis=0):
    return tgt.clone().index_add_(axis, idx, src)
def _segment_sum(src, seg, n):
    out = torch.zeros((n,) + src.shape[1:], dtype=src.dtype)
    return out.index_add_(0, seg, src)
def _pad(x, pw):
    flat = tuple(v for pair in reversed(pw) for v in pair)
    return torch.nn.functional.pad(x, flat)

kernel_name = 'recurrent_scan__sliding_local_dilated__dense_activation_relu__prompt'

def setup_inputs():

    _w = (lambda a: (lambda t: t.to(torch.bfloat16) if t.is_floating_point() else t)(torch.as_tensor(a)))
    def _A_setup():
        B, S, D = 1, 2048, 576
        return {
            "x": _w((lambda *s: torch.randn(*s, dtype=torch.bfloat16) * 0.05)(B, S, D)),
            "wqkv": _w((lambda *s: torch.randn(*s, dtype=torch.bfloat16) * 0.05)(D, 3 * D)),
            "wo": _w((lambda *s: torch.randn(*s, dtype=torch.bfloat16) * 0.05)(D, D)),
        }
    _A_in = _A_setup()

    def _B_setup():
        B, S, D, Dff = 1, 2048, 576, 4608
        return {
            "x": _w((lambda *s: torch.randn(*s, dtype=torch.bfloat16) * 0.05)(B, S, D)),
            "w1": _w((lambda *s: torch.randn(*s, dtype=torch.bfloat16) * 0.05)(D, Dff)),
            "w2": _w((lambda *s: torch.randn(*s, dtype=torch.bfloat16) * 0.05)(Dff, D)),
        }
    _B_in = _B_setup()

    return {
        "A_x": _A_in["x"],
        "A_wqkv": _A_in["wqkv"],
        "A_wo": _A_in["wo"],
        "B_x": _B_in["x"],
        "B_w1": _B_in["w1"],
        "B_w2": _B_in["w2"],
    }

def reference(A_x, A_wqkv, A_wo, B_x, B_w1, B_w2):
    def _A_ref(x, wqkv, wo):
        B, S, D = x.shape
        H = 32; Dh = D // H
        W, dil = 8, 2
        qkv = (lambda x, *d: x.permute(*d))((x @ wqkv).reshape(B, S, 3, H, Dh), 2, 0, 3, 1, 4)
        q, k, v = qkv[0], qkv[1], qkv[2]
        scale = Dh ** -0.5
        pos = torch.arange(S)
        off = torch.arange(W) * dil
        idx = pos[:, None] - off[None, :]
        mask = idx >= 0
        idx_c = torch.where(mask, idx, 0).reshape(-1)
        kg = k[:, :, idx_c, :].reshape(B, H, S, W, Dh)
        vg = v[:, :, idx_c, :].reshape(B, H, S, W, Dh)
        logits = torch.einsum("bhsd,bhswd->bhsw", q, kg) * scale
        logits = torch.where(mask[None, None, :, :], logits, -1e9)
        attn = _softmax(logits, axis=-1)
        o = torch.einsum("bhsw,bhswd->bhsd", attn, vg)
        return (lambda x, *d: x.permute(*d))(o, 0, 2, 1, 3).reshape(B, S, D) @ wo

    def _B_ref(x, w1, w2):
        h = torch.einsum("bsd,df->bsf", x, w1)
        zero = torch.zeros_like(h)
        h = torch.maximum(h, zero)
        return torch.einsum("bsf,fd->bsd", h, w2)

    x = A_x
    xn = _layernorm(x, axis=-1)
    s = _A_ref(xn, A_wqkv, A_wo)
    g = _silu(_B_ref(xn, B_w1, B_w2))
    return x + s * g
```

```cpp
#include <hip/hip_runtime.h>
#include <hip/hip_bf16.h>
#include <stdint.h>

typedef unsigned short u16;
__device__ __forceinline__ float bf2f(u16 v) { return __uint_as_float(((unsigned)v) << 16); }
__device__ __forceinline__ u16 f2bf(float f) { __bf16 b = (__bf16)f; return __builtin_bit_cast(u16, b); }
__device__ __forceinline__ float rbf(float f) { return bf2f(f2bf(f)); }

constexpr int S = 2048, D = 576, DFF = 4608, H = 32, DH = 18;

__global__ void nv_ln(const u16* x, u16* xn) {
  int row = blockIdx.x; int lane = threadIdx.x;
  float v[9]; float s = 0;
  for (int i = 0; i < 9; ++i) { v[i] = bf2f(x[row * D + lane + 64 * i]); s += v[i]; }
  for (int o = 32; o; o >>= 1) s += __shfl_xor(s, o);
  float m = s / D; float q = 0;
  for (int i = 0; i < 9; ++i) { float d = v[i] - m; q += d * d; }
  for (int o = 32; o; o >>= 1) q += __shfl_xor(q, o);
  float var = rbf(q / D); float mb = rbf(m);
  float den = rbf(sqrtf(rbf(var + 1e-5f)));
  for (int i = 0; i < 9; ++i) xn[row * D + lane + 64 * i] = f2bf(rbf(v[i] - mb) / den);
}

template <bool RELU>
__global__ void nv_gemm(const u16* A, const u16* B, u16* C, int M, int N, int K) {
  __shared__ float As[32][33], Bs[32][33];
  int tx = threadIdx.x & 31, ty = threadIdx.x >> 5;
  int r0 = blockIdx.y * 32, c0 = blockIdx.x * 32;
  float acc[4] = {0, 0, 0, 0};
  for (int k0 = 0; k0 < K; k0 += 32) {
    for (int i = 0; i < 4; ++i) {
      As[ty + 8 * i][tx] = bf2f(A[(size_t)(r0 + ty + 8 * i) * K + k0 + tx]);
      Bs[ty + 8 * i][tx] = bf2f(B[(size_t)(k0 + ty + 8 * i) * N + c0 + tx]);
    }
    __syncthreads();
    for (int k = 0; k < 32; ++k) for (int i = 0; i < 4; ++i) acc[i] += As[ty + 8 * i][k] * Bs[k][tx];
    __syncthreads();
  }
  for (int i = 0; i < 4; ++i) { float v = acc[i]; if (RELU) v = fmaxf(v, 0.f); C[(size_t)(r0 + ty + 8 * i) * N + c0 + tx] = f2bf(v); }
}

__global__ void nv_attn(const u16* qkv, u16* o) {
  int idx = blockIdx.x * blockDim.x + threadIdx.x;
  int s = idx / H, h = idx % H;
  float q[DH]; for (int d = 0; d < DH; ++d) q[d] = bf2f(qkv[(size_t)s * 3 * D + h * DH + d]);
  float lg[8]; float mx = -3e38f;
  const float scale = 0.23570226039551584f;
  for (int w = 0; w < 8; ++w) {
    int t = s - 2 * w; float l;
    if (t >= 0) { float a = 0; for (int d = 0; d < DH; ++d) a += q[d] * bf2f(qkv[(size_t)t * 3 * D + D + h * DH + d]); l = rbf(rbf(a) * scale); }
    else l = rbf(-1e9f);
    lg[w] = l; mx = fmaxf(mx, l);
  }
  float sum = 0; for (int w = 0; w < 8; ++w) { lg[w] = __expf(lg[w] - mx); sum += lg[w]; }
  float acc[DH]; for (int d = 0; d < DH; ++d) acc[d] = 0;
  for (int w = 0; w < 8; ++w) { int t = s - 2 * w; if (t < 0) t = 0; float p = rbf(lg[w] / sum);
    for (int d = 0; d < DH; ++d) acc[d] += p * bf2f(qkv[(size_t)t * 3 * D + 2 * D + h * DH + d]); }
  for (int d = 0; d < DH; ++d) o[(size_t)s * D + h * DH + d] = f2bf(acc[d]);
}

__global__ void nv_final(const u16* x, const u16* sm, const u16* f, u16* out) {
  int i = blockIdx.x * blockDim.x + threadIdx.x;
  float fv = bf2f(f[i]); float g = rbf(fv / (1.f + __expf(-fv)));
  out[i] = f2bf(bf2f(x[i]) + rbf(bf2f(sm[i]) * g));
}

extern "C" void kernel_launch(void* const* d_in, const int* in_sizes, int n_in, void* d_out, int out_size, void* d_ws, size_t ws_size, hipStream_t stream) {
  const u16* x = (const u16*)d_in[0]; const u16* wqkv = (const u16*)d_in[1]; const u16* wo = (const u16*)d_in[2];
  const u16* w1 = (const u16*)d_in[4]; const u16* w2 = (const u16*)d_in[5];
  u16* ws = (u16*)d_ws;
  u16* xn = ws; u16* qkv = xn + S * D; u16* o = qkv + S * 3 * D; u16* sm = o + S * D; u16* f = sm + S * D; u16* h = f + S * D;
  nv_ln<<<S, 64, 0, stream>>>(x, xn);
  nv_gemm<false><<<dim3(3 * D / 32, S / 32), 256, 0, stream>>>(xn, wqkv, qkv, S, 3 * D, D);
  nv_attn<<<S * H / 256, 256, 0, stream>>>(qkv, o);
  nv_gemm<false><<<dim3(D / 32, S / 32), 256, 0, stream>>>(o, wo, sm, S, D, D);
  nv_gemm<true><<<dim3(DFF / 32, S / 32), 256, 0, stream>>>(xn, w1, h, S, DFF, D);
  nv_gemm<false><<<dim3(D / 32, S / 32), 256, 0, stream>>>(h, w2, f, S, D, DFF);
  nv_final<<<S * D / 256, 256, 0, stream>>>(x, sm, f, (u16*)d_out);
}
```

```cpp
#include <hip/hip_runtime.h>
#include <utility>
#include <type_traits>
#define XROT 0
#define ABL 0
#define NFIRST 15
#define PAUX "sc1"
#define GRAY 0
typedef unsigned short u16;
typedef unsigned u32;
typedef short s16x2 __attribute__((ext_vector_type(2)));
typedef short s16x4 __attribute__((ext_vector_type(4)));
typedef short s16x8 __attribute__((ext_vector_type(8)));
typedef __bf16 bf16x8 __attribute__((ext_vector_type(8)));
typedef __bf16 bf16x2 __attribute__((ext_vector_type(2)));
typedef float f32x2 __attribute__((ext_vector_type(2)));
typedef float f32x4 __attribute__((ext_vector_type(4)));
typedef u32 u32x2 __attribute__((ext_vector_type(2)));
typedef u32 u32x4 __attribute__((ext_vector_type(4)));
#define LDS3 __attribute__((address_space(3)))
#define DI __device__ __forceinline__
#define SB0() __builtin_amdgcn_sched_barrier(0)
#define IC(x) std::integral_constant<int, (x)>{}
template <int... Is, class F> DI void sfor_impl(std::integer_sequence<int, Is...>, F&& f) { (f(std::integral_constant<int, Is>{}), ...); }
template <int N, class F> DI void sfor(F&& f) { sfor_impl(std::make_integer_sequence<int, N>{}, f); }
typedef __amdgpu_buffer_rsrc_t srd_t;

constexpr int SEQ = 2048, DM = 576, DFF = 4608;
constexpr u32 XROW = 1152, W1ROW = 9216, WQROW = 3456, BIAS = 4096, OOB = 0x80000000u;
constexpr u32 WS_EPOCH = 0;
constexpr u32 WS_FLAG = 4096;
constexpr u32 WS_HFLAG = 16384;
constexpr u32 WS_HALO = 32768;
constexpr u32 WS_P = 2u << 20;
constexpr u32 WS_H = 21u << 20;
constexpr u32 WS_END = WS_H + 16u * 12 * 4 * 24576;
constexpr u32 WS_STAMP = 64u << 20;
constexpr u32 FSLOT = 24576, FNS = 5, ASLOT = 28672, ANS = 4, XBASE = 122880, TBASE = 155648, SBASE = 156672, LDS_BYTES = 157696;
constexpr u32 VROW = 296;
constexpr u32 SPIN_MAX = 1u << 16;

DI srd_t mksrd(const void* p, u32 n) { return __builtin_amdgcn_make_buffer_rsrc((void*)p, (short)0, (int)n, 0x00020000); }
DI srd_t mksrd_b(const void* p, u32 n) { return mksrd((const char*)p - BIAS, n + BIAS); }
DI void setm0(u32 v) { asm volatile("s_mov_b32 m0, %0\n\ts_nop 0" :: "s"(v) : "memory"); }
template <int B> DI void setm0_add(u32 a) { asm volatile("s_add_u32 m0, %0, %1" :: "s"(a), "i"(B) : "memory", "scc"); }
template <int OFF> DI void dma16(u32 voff, srd_t srd, u32 soff) {
  asm volatile("buffer_load_dwordx4 %0, %1, %2 offen offset:%3 lds" :: "v"(voff), "s"(srd), "s"(soff), "i"(OFF) : "memory");
}
template <int OFF> DI void ld128_asm(u32x4& d, u32 voff, srd_t srd, u32 soff) {
  asm volatile("buffer_load_dwordx4 %0, %1, %2, %3 offen offset:%4" : "=v"(d) : "v"(voff), "s"(srd), "s"(soff), "i"(OFF) : "memory");
}
DI void touch(u32 voff, srd_t srd, u32 soff) { if (ABL & 1) voff = OOB; asm volatile("buffer_load_dword %0, %1, %2 offen lds" :: "v"(voff), "s"(srd), "s"(soff) : "memory"); }
DI void poll_issue(u32& d, u32 voff, srd_t srd, u32 soff) { asm volatile("buffer_load_dword %0, %1, %2, %3 offen sc1" : "=v"(d) : "v"(voff), "s"(srd), "s"(soff) : "memory"); }
template <int N> DI void waitvm() { asm volatile("s_waitcnt vmcnt(%0)" :: "i"(N) : "memory"); }
template <int N> DI void waitvm_v(u32& d) { asm volatile("s_waitcnt vmcnt(%1)" : "+v"(d) : "i"(N) : "memory"); }
template <int N> DI void wait9(u32x4 (&p)[9]) {
  asm volatile("s_waitcnt vmcnt(%9)" : "+v"(p[0]), "+v"(p[1]), "+v"(p[2]), "+v"(p[3]), "+v"(p[4]), "+v"(p[5]), "+v"(p[6]), "+v"(p[7]), "+v"(p[8]) : "i"(N) : "memory");
}
template <int N> DI void waitlgkm() { __builtin_amdgcn_s_waitcnt(0xC07F | (N << 8)); }
DI s16x4 trrd(u32 a) { return __builtin_amdgcn_ds_read_tr16_b64_v4i16((LDS3 s16x4*)(size_t)a); }
DI u32x4 rd128(u32 a) { return *(LDS3 u32x4*)(size_t)a; }
DI void wr128(u32 a, u32x4 v) { *(LDS3 u32x4*)(size_t)a = v; }
DI void wr64(u32 a, u32x2 v) { *(LDS3 u32x2*)(size_t)a = v; }
DI u32 cvtpk(float lo, float hi) { f32x2 v = {lo, hi}; return __builtin_bit_cast(u32, __builtin_convertvector(v, bf16x2)); }
DI float bflo(u32 d) { return __uint_as_float(d << 16); }
DI float bfhi(u32 d) { return __uint_as_float(d & 0xffff0000u); }
DI float rbf(float f) { return bflo(cvtpk(f, 0.f)); }
DI float dot2(u32 a, u32 b, float c) { return __builtin_amdgcn_fdot2_f32_bf16(__builtin_bit_cast(bf16x2, a), __builtin_bit_cast(bf16x2, b), c, false); }
DI s16x8 cat(s16x4 lo, s16x4 hi) { return __builtin_shufflevector(lo, hi, 0, 1, 2, 3, 4, 5, 6, 7); }
DI f32x4 mfma(s16x8 a, u32x4 b, f32x4 c) { return __builtin_amdgcn_mfma_f32_16x16x32_bf16(__builtin_bit_cast(bf16x8, a), __builtin_bit_cast(bf16x8, b), c, 0, 0, 0); }
DI void mfma_v(f32x4& c, s16x8 a, u32x4 b) { asm volatile("v_mfma_f32_16x16x32_bf16 %0, %1, %2, %0" : "+v"(c) : "v"(a), "v"(b)); }
DI void mfma_v0(f32x4& c, s16x8 a, u32x4 b) { asm volatile("v_mfma_f32_16x16x32_bf16 %0, %1, %2, 0" : "=&v"(c) : "v"(a), "v"(b)); }
DI void mfma_a(f32x4& c, s16x8 a, u32x4 b) { asm volatile("v_mfma_f32_16x16x32_bf16 %0, %1, %2, %0" : "+v"(c) : "v"(a), "a"(b)); }
DI void mfma_a0(f32x4& c, s16x8 a, u32x4 b) { asm volatile("v_mfma_f32_16x16x32_bf16 %0, %1, %2, 0" : "=&v"(c) : "v"(a), "a"(b)); }
DI void mfma_acc(f32x4& c, s16x8 a, u32x4 b) { asm volatile("v_mfma_f32_16x16x32_bf16 %0, %1, %2, %0" : "+a"(c) : "v"(a), "v"(b)); }
DI void mfma_acc_a(f32x4& c, s16x8 a, u32x4 b) { asm volatile("v_mfma_f32_16x16x32_bf16 %0, %1, %2, %0" : "+a"(c) : "a"(a), "v"(b)); }
DI void pin_a(f32x4& a) { asm volatile("" : "+a"(a)); }
DI void mfma_av(f32x4& c, s16x8 a, u32x4 b) { asm volatile("v_mfma_f32_16x16x32_bf16 %0, %1, %2, %0" : "+v"(c) : "a"(a), "v"(b)); }
DI void mfma_av0(f32x4& c, s16x8 a, u32x4 b) { asm volatile("v_mfma_f32_16x16x32_bf16 %0, %1, %2, 0" : "=&v"(c) : "a"(a), "v"(b)); }
DI void mfma_aa(f32x4& c, s16x8 a, u32x4 b) { asm volatile("v_mfma_f32_16x16x32_bf16 %0, %1, %2, %0" : "+v"(c) : "a"(a), "a"(b)); }
DI void mfma_aa0(f32x4& c, s16x8 a, u32x4 b) { asm volatile("v_mfma_f32_16x16x32_bf16 %0, %1, %2, 0" : "=&v"(c) : "a"(a), "a"(b)); }
template <int OFF> DI void trrd_a(s16x4& d, u32 a) { asm volatile("ds_read_b64_tr_b16 %0, %1 offset:%2" : "=a"(d) : "v"(a), "i"(OFF) : "memory"); }
template <int OFF> DI void trrd_v(s16x4& d, u32 a) { asm volatile("ds_read_b64_tr_b16 %0, %1 offset:%2" : "=v"(d) : "v"(a), "i"(OFF) : "memory"); }
template <int OFF> DI void rd128_v(u32x4& d, u32 a) { asm volatile("ds_read_b128 %0, %1 offset:%2" : "=v"(d) : "v"(a), "i"(OFF) : "memory"); }
template <int OFF> DI void ld128_a(u32x4& d, u32 voff, srd_t srd, u32 soff) {
  asm volatile("buffer_load_dwordx4 %0, %1, %2, %3 offen offset:%4 " PAUX : "=a"(d) : "v"(voff), "s"(srd), "s"(soff), "i"(OFF) : "memory"); }
template <int N> DI void wl() { asm volatile("s_waitcnt lgkmcnt(%0)" :: "i"(N) : "memory"); }
DI void zero_v(f32x4& a) { f32x2 lo, hi; asm volatile("v_mov_b64 %0, 0\n\tv_mov_b64 %1, 0" : "=v"(lo), "=v"(hi)); a = __builtin_shufflevector(lo, hi, 0, 1, 2, 3); }
DI float fmax2(float a, float b) { return __builtin_elementwise_maximum(a, b); }
template <int R> struct FR;
#define DEF_FR(R, A0, A1, A2, A3) template <> struct FR<R> { \
  template <int OFF> static DI void lo(s16x4& d, u32 a) { asm volatile("ds_read_b64_tr_b16 %0, %1 offset:%2" : "={a[" #A0 ":" #A1 "]}"(d) : "v"(a), "i"(OFF) : "memory"); } \
  template <int OFF> static DI void hi(s16x4& d, u32 a) { asm volatile("ds_read_b64_tr_b16 %0, %1 offset:%2" : "={a[" #A2 ":" #A3 "]}"(d) : "v"(a), "i"(OFF) : "memory"); } \
  static DI void vv(f32x4& c, s16x4 l, s16x4 h, u32x4 b) { asm volatile("v_mfma_f32_16x16x32_bf16 %0, a[" #A0 ":" #A3 "], %3, %0" : "+v"(c) : "{a[" #A0 ":" #A1 "]}"(l), "{a[" #A2 ":" #A3 "]}"(h), "v"(b)); } \
  static DI void vv0(f32x4& c, s16x4 l, s16x4 h, u32x4 b) { asm volatile("v_mfma_f32_16x16x32_bf16 %0, a[" #A0 ":" #A3 "], %3, 0" : "=&v"(c) : "{a[" #A0 ":" #A1 "]}"(l), "{a[" #A2 ":" #A3 "]}"(h), "v"(b)); } \
  static DI void va(f32x4& c, s16x4 l, s16x4 h, u32x4 b) { asm volatile("v_mfma_f32_16x16x32_bf16 %0, a[" #A0 ":" #A3 "], %3, %0" : "+v"(c) : "{a[" #A0 ":" #A1 "]}"(l), "{a[" #A2 ":" #A3 "]}"(h), "a"(b)); } \
  static DI void va0(f32x4& c, s16x4 l, s16x4 h, u32x4 b) { asm volatile("v_mfma_f32_16x16x32_bf16 %0, a[" #A0 ":" #A3 "], %3, 0" : "=&v"(c) : "{a[" #A0 ":" #A1 "]}"(l), "{a[" #A2 ":" #A3 "]}"(h), "a"(b)); } \
  static DI void k16(f32x4& c, s16x4 l, u32x2 b) { asm volatile("v_mfma_f32_16x16x16_bf16 %0, a[" #A0 ":" #A1 "], %2, %0" : "+v"(c) : "{a[" #A0 ":" #A1 "]}"(l), "v"(b)); } \
  static DI void av(f32x4& c, s16x4 l, s16x4 h, u32x4 b) { asm volatile("v_mfma_f32_16x16x32_bf16 %0, a[" #A0 ":" #A3 "], %3, %0" : "+a"(c) : "{a[" #A0 ":" #A1 "]}"(l), "{a[" #A2 ":" #A3 "]}"(h), "v"(b)); } };
DEF_FR(0, 208, 209, 210, 211)
DEF_FR(1, 212, 213, 214, 215)
DEF_FR(2, 216, 217, 218, 219)
DEF_FR(3, 220, 221, 222, 223)
DEF_FR(4, 224, 225, 226, 227)
DEF_FR(5, 228, 229, 230, 231)
DEF_FR(6, 232, 233, 234, 235)
DEF_FR(7, 236, 237, 238, 239)
DEF_FR(8, 240, 241, 242, 243)
DEF_FR(9, 244, 245, 246, 247)
DEF_FR(10, 248, 249, 250, 251)
DEF_FR(11, 252, 253, 254, 255)
DI void mfma_vn(f32x4& c, s16x8 a, u32x4 b) { asm volatile("s_nop 1\n\tv_mfma_f32_16x16x32_bf16 %0, %1, %2, %0" : "+v"(c) : "v"(a), "v"(b)); }
DI void mfma_v0n(f32x4& c, s16x8 a, u32x4 b) { asm volatile("s_nop 1\n\tv_mfma_f32_16x16x32_bf16 %0, %1, %2, 0" : "=&v"(c) : "v"(a), "v"(b)); }
DI void mfma16_c(f32x4& d, u32x2 a, u32x2 b, f32x4 c) { asm volatile("v_mfma_f32_16x16x16_bf16 %0, %1, %2, %3" : "=&v"(d) : "v"(a), "v"(b), "v"(c)); }
DI void mfma16_v(f32x4& d, u32x2 a, u32x2 b) { asm volatile("v_mfma_f32_16x16x16_bf16 %0, %1, %2, %0" : "+v"(d) : "v"(a), "v"(b)); }
template <int CTRL> DI float dppadd(float v) { return v + __int_as_float(__builtin_amdgcn_update_dpp(0, __float_as_int(v), CTRL, 0xf, 0xf, true)); }
DI u32 v_lshl16(u32 a) { u32 r; asm volatile("v_lshlrev_b32 %0, 16, %1" : "=v"(r) : "v"(a)); return r; }
DI u32 v_andhi(u32 a) { u32 r; asm volatile("v_and_b32 %0, 0xffff0000, %1" : "=v"(r) : "v"(a)); return r; }
DI u32 v_fmav(u32 a, float b, float c) { u32 r; asm volatile("v_fma_f32 %0, %1, %2, %3" : "=v"(r) : "v"(a), "v"(b), "v"(c)); return r; }
DI u32 v_cvtv(u32 a, u32 b) { u32 r; asm volatile("v_cvt_pk_bf16_f32 %0, %1, %2" : "=v"(r) : "v"(a), "v"(b)); return r; }
DI u32 v_cvtf(float a, float b) { u32 r; asm volatile("v_cvt_pk_bf16_f32 %0, %1, %2" : "=v"(r) : "v"(a), "v"(b)); return r; }
DI u32 v_addv(u32 a, u32 s) { u32 r; asm volatile("v_add_u32 %0, %1, %2" : "=v"(r) : "s"(s), "v"(a)); return r; }
DI void v_swap(u32& a, u32& b) { asm volatile("v_swap_b32 %0, %1" : "+v"(a), "+v"(b)); }
DI void keep4(u32x4 a, u32x4 b, u32x4 c, u32x4 d) { asm volatile("" :: "v"(a), "v"(b), "v"(c), "v"(d)); }
DI void pin(f32x4& a) { asm volatile("" : "+v"(a)); }
DI void pin2(float& a, float& b) { asm volatile("" : "+v"(a), "+v"(b)); }
DI void zero_a(f32x4& a) { a = f32x4{0, 0, 0, 0}; asm volatile("" : "+a"(a)); }
DI u32 sopaque(u32 v) { asm volatile("" : "+s"(v)); return v; }
DI float max2(float a, float b) { float r; asm("v_max_f32 %0, %1, %2" : "=v"(r) : "v"(a), "v"(b)); return r; }
DI float max3(float a, float b, float c) { float r; asm("v_max3_f32 %0, %1, %2, %3" : "=v"(r) : "v"(a), "v"(b), "v"(c)); return r; }
DI void pin1(float& a) { asm volatile("" : "+v"(a)); }
DI void pinu(u32x4& a) { asm volatile("" : "+v"(a)); }
DI void pinu2(u32x2& a, u32x2& b) { asm volatile("" : "+v"(a), "+v"(b)); }
DI void undef_v(f32x4& a) { asm volatile("" : "=v"(a)); }
DI void undef_a(u32x4& a) { asm volatile("" : "=a"(a)); }
DI void pinw(u32& a) { asm volatile("" : "+v"(a)); }
DI void wr64_a(u32 a, u32x4 d) {
  asm volatile("ds_write_b64 %0, %1\n\tds_write_b64 %0, %2 offset:%3" :: "v"(a), "a"(__builtin_shufflevector(d, d, 0, 1)), "a"(__builtin_shufflevector(d, d, 2, 3)), "i"(80 * VROW) : "memory"); }
DI void mfma2_v(f32x4& c, s16x4 l, s16x4 h, u32x4 b) { mfma_v(c, cat(l, h), b); }
DI void mfma2_v0(f32x4& c, s16x4 l, s16x4 h, u32x4 b) { mfma_v0(c, cat(l, h), b); }
DI void ld128_vs(u32x4& d, u32 voff, srd_t srd, u32 soff) { asm volatile("buffer_load_dwordx4 %0, %1, %2, %3 offen sc1" : "=v"(d) : "v"(voff), "s"(srd), "s"(soff) : "memory"); }
DI void ld64_v(u32x2& d, u32 voff, srd_t srd, u32 soff) { asm volatile("buffer_load_dwordx2 %0, %1, %2, %3 offen" : "=v"(d) : "v"(voff), "s"(srd), "s"(soff) : "memory"); }
DI void pinw_a(u32& a) { asm volatile("" : "+a"(a)); }
DI u32x2 rd64(u32 a) { return *(LDS3 u32x2*)(size_t)a; }
DI void pins(s16x8& a) { asm volatile("" : "+v"(a)); }
DI void nop8(f32x4 (&a)[4][2]) { asm volatile("s_nop 7" : "+v"(a[0][0]), "+v"(a[0][1]), "+v"(a[1][0]), "+v"(a[1][1]), "+v"(a[2][0]), "+v"(a[2][1]), "+v"(a[3][0]), "+v"(a[3][1])); }
DI u32 rfl(u32 v) { return __builtin_amdgcn_readfirstlane(v); }
DI void st128(u32x4 d, srd_t s, u32 v, u32 so) { if (ABL & 4) v = OOB; if (ABL & 8) { __builtin_amdgcn_raw_buffer_store_b128(d, s, v, so, 0); return; } __builtin_amdgcn_raw_buffer_store_b128(d, s, v, so, 16); }
DI u32x4 ld128(srd_t s, u32 v, u32 so) { return __builtin_amdgcn_raw_buffer_load_b128(s, v, so, 16); }
DI void st_flag(u32 val, srd_t s, u32 v0, u32 so) { __builtin_amdgcn_raw_buffer_store_b32(val, s, v0, so, 16); }
DI bool flags_ok(u32 word, u32 tag, u32 need) { const u32 d = word - tag; return __all(need == 0 || (d < 16u && d >= need)); }
DI void spin(u32 voff, srd_t s, u32 so, u32 tag, u32 need) {
#pragma unroll 1
  for (u32 i = 0; i < SPIN_MAX; ++i) { u32 wd; poll_issue(wd, voff, s, so); waitvm_v<0>(wd); if (flags_ok(wd, tag, need)) break; __builtin_amdgcn_s_sleep(1); }
}
DI void spin2(u32 voff, srd_t s, u32 so, u32 lo, u32 lim) {
#pragma unroll 1
  for (u32 i = 0; i < SPIN_MAX; ++i) { __builtin_amdgcn_s_sleep(1); u32 wd; poll_issue(wd, voff, s, so); waitvm_v<0>(wd); if (__all(wd - lo <= lim)) break; }
}
DI void spin2n(u32 voff, srd_t s, u32 so, u32 lo, u32 lim) {
#pragma unroll 1
  for (u32 i = 0; i < SPIN_MAX; ++i) { u32 wd; poll_issue(wd, voff, s, so); waitvm_v<0>(wd); if (__all(wd - lo <= lim)) break; __builtin_amdgcn_s_sleep(1); }
}
#define STAMP(i)
#define STAMP_DECL
#define STAMP_FLUSH()

enum { OP_W = 1, OP_HS, OP_HB, OP_HC, OP_FS, OP_FLAG, OP_POLL, OP_MISC, OP_SL, OP_PL };
constexpr int dpos(int m) { return m == 3 ? 0 : m == 9 ? 1 : m == 15 ? 2 : m == 21 ? 3 : m == 27 ? 4 : m == 30 ? 5 : -1; }
constexpr int fpos(int m) { return (m == 6 || m == 12 || m == 18 || m == 24) ? m / 6 - 1 : (m >= 35 && m % 4 == 3) ? 4 + (m - 35) / 4 : -1; }
constexpr int f2op(bool O, int J, int m) {
  if (J <= 14 && dpos(m) >= 0) return OP_W;
  if (J == -2) return (m == 34 || m == 37 || m == 40 || m == 43 || m == 46) ? OP_MISC : 0;
  if (J == -1) return m == 46 ? OP_MISC : 0;
  const int p = fpos(m);
  if (J <= 5 && (p == 0 || p == 2 || p == 5)) return OP_HS;
  if ((J == 2 && m == 47) || (J == 6 && m == 47) || (J == 15 && m == 12) || (J == 17 && m == 10)) return OP_FLAG;
  if ((J == 3 && m == 43) || (J == 7 && m == 47)) return OP_POLL;
  if (O && ((J == 12 && m == 33) || (J == 15 && m == 44) || (J == 17 && m == 40))) return OP_POLL;
  if ((J == 4 && (p == 4 || p == 6 || p == 7) ) || (J == 4 && m == 33) || (J == 5 && (p == 1 || p == 3 || p == 4 || p == 6 || p == 7 || m == 1 || m == 33 || m == 45))) return OP_HB;
  if ((J == 6 && p >= 0 && p < 7) || (J == 7 && p >= 0 && p < 5)) return OP_HC;
  if ((J == 8 && p >= 4) || (J == 9 && p >= 0)) return OP_HB;
  if ((J == 10 && p >= 0) || (J == 11 && p >= 0 && p < 4)) return OP_HC;
  if ((J == 14 || J == 16) && (m == 2 || m == 5 || m == 8 || m == 11)) return OP_FS;
  if (O && ((J == 13 && p >= 4) || (J == 14 && (p >= 2 || m == 33)) || (J == 15 && m >= 15 && m < 30 && m % 3 == 0))) return OP_SL;
  if (O && J == 17 && m >= 13 && m < 37 && m % 3 == 1) return OP_PL;
  if (O && J == 16 && m >= 16 && m < 32 && m % 4 == 0) return OP_MISC;
  return 0;
}
constexpr int f2count(bool O, int J0, int m0, int J1, int m1) {
  int n = 0;
  for (int J = J0; J <= J1; ++J) for (int m = 0; m < 48; ++m) { if (J == J0 && m <= m0) continue; if (J == J1 && m >= m1) continue; n += f2op(O, J, m) != 0; }
  if (J0 < 0 && J1 >= 0) n += 6;
  return n > 63 ? 63 : n;
}
constexpr int f2index(bool O, int J, int m) {
  int n = 0; const int k = f2op(O, J, m);
  for (int j = 0; j <= J; ++j) for (int q = 0; q < 48; ++q) { if (j == J && q >= m) break; n += f2op(O, j, q) == k; }
  return n;
}
constexpr int f2find(bool O, int k, int idx) {
  int n = 0;
  for (int j = 0; j < 18; ++j) for (int q = 0; q < 48; ++q) if (f2op(O, j, q) == k) { if (n == idx) return 48 * j + q; ++n; }
  return -1;
}
constexpr int f2after(bool O, int k, int idx, int J, int m) { return f2count(O, f2find(O, k, idx) / 48, f2find(O, k, idx) % 48, J, m); }
constexpr int imin(int a, int b) { return a < b ? a : b; }
static_assert(f2find(1, OP_HS, 17) >= 0 && f2find(1, OP_HS, 18) < 0 && f2find(1, OP_SL, 15) >= 0 && f2find(1, OP_SL, 16) < 0 && f2find(1, OP_PL, 7) >= 0 && f2find(1, OP_PL, 8) < 0 &&
              f2find(1, OP_HB, 23) >= 0 && f2find(1, OP_HB, 24) < 0 && f2find(1, OP_HC, 23) >= 0 && f2find(1, OP_HC, 24) < 0 && f2find(1, OP_FS, 7) >= 0, "schedule");

struct XState { u32 vx[4]; srd_t srd; float rs[2], sh[2]; u32 bx; };
DI void x_dma(const XState& X, u32 ldsdst, u32 soff) {
  setm0(ldsdst);
  sfor<4>([&](auto P) { dma16<P.value * 1024>(X.vx[P.value], X.srd, soff); });
}
DI u32x4 norm_frag(u32x4 r, float rs, float sh) {
  u32x4 o;
#pragma unroll
  for (int i = 0; i < 4; ++i) o[i] = cvtpk(__builtin_fmaf(bflo(r[i]), rs, sh), __builtin_fmaf(bfhi(r[i]), rs, sh));
  return o;
}
template <int K> DI void norm_op(const XState& X, const u32x4 (&raw)[2], u32x4 (&Bn)[2], u32 (&tlo)[2], u32 (&thi)[2]) {
  constexpr int blk = K / 10, r = K % 10, i = 2 * blk + (r & 1), tb = i >> 2, d = i & 3, op = r >> 1, z = r & 1;
  if constexpr (op == 0) tlo[z] = v_lshl16(raw[tb][d]);
  if constexpr (op == 1) thi[z] = v_andhi(raw[tb][d]);
  if constexpr (op == 2) tlo[z] = v_fmav(tlo[z], X.rs[tb], X.sh[tb]);
  if constexpr (op == 3) thi[z] = v_fmav(thi[z], X.rs[tb], X.sh[tb]);
  if constexpr (op == 4) Bn[tb][d] = v_cvtv(tlo[z], thi[z]);
}
template <int N> DI void ln_group(u32x4 (&p)[9], u32 dst, u32 xw) {
  wait9<N>(p);
  wr128(xw, p[0]); wr128(xw + 4096, p[1]);
  float a0 = 0, a1 = 0, b0 = 0, b1 = 0;
#pragma unroll
  for (int c = 0; c < 9; ++c) { a0 = dot2(p[c][0], 0x3f803f80u, a0); a1 = dot2(p[c][1], 0x3f803f80u, a1); a0 = dot2(p[c][2], 0x3f803f80u, a0); a1 = dot2(p[c][3], 0x3f803f80u, a1);
    b0 = dot2(p[c][0], p[c][0], b0); b1 = dot2(p[c][1], p[c][1], b1); b0 = dot2(p[c][2], p[c][2], b0); b1 = dot2(p[c][3], p[c][3], b1); }
  const float a = dppadd<0x141>(dppadd<0x4E>(dppadd<0xB1>(a0 + a1))), b = dppadd<0x141>(dppadd<0x4E>(dppadd<0xB1>(b0 + b1)));
  const float m = a * (1.f / 576.f), var = fmaxf(b * (1.f / 576.f) - m * m, 0.f);
  const float rs = __builtin_amdgcn_rcpf(rbf(__builtin_amdgcn_sqrtf(rbf(rbf(var) + 1e-5f))));
  wr64(dst, u32x2{__float_as_uint(rs), __float_as_uint(-rbf(m) * rs)});
  asm volatile("" ::: "memory");
}
DI void epoch_issue(u32& e, const char* p) { asm volatile("s_load_dword %0, %1, 0x0" : "=s"(e) : "s"(p) : "memory"); }
DI void epoch_wait(u32& e0, u32& e1) { asm volatile("s_waitcnt lgkmcnt(0)" : "+s"(e0), "+s"(e1) :: "memory"); }

__global__ __launch_bounds__(256, 1) __attribute__((flatten)) void fused(const u16* __restrict__ x, const u16* __restrict__ wqkv, const u16* __restrict__ wo, const u16* __restrict__ w1,
                                               const u16* __restrict__ w2, u16* __restrict__ out, char* __restrict__ ws) {
  extern __shared__ __attribute__((aligned(16))) char smem[];
  STAMP_DECL;
  STAMP(0);
  const u32 ldsb = (u32)(size_t)(LDS3 char*)smem;
  const u32 l = threadIdx.x & 63, w = rfl(threadIdx.x >> 6);
  const u32 bid = blockIdx.x, xcd = bid & 7, jj = bid >> 3;
  const u32 t7 = jj & 7, tile = (xcd & 1) * 8 + t7, sq = xcd >> 1, role = jj >> 3;
  const u32 tok0 = tile * 128;
  const u32 r8 = l >> 3, s8 = l & 7;
  const u32 g = l >> 4, tk = l & 15, q4 = (l >> 2) & 3, p4 = l & 3, rr = 4 * (g & 1) + q4, hh = g >> 1;
  XState X;
  X.srd = mksrd_b(x, SEQ * XROW);
  const u32 pa0 = BIAS + (tok0 + 32 * w + 2 * r8) * XROW + 16 * s8;
  auto grp_so = [&](int i) { const u32 p = (i + XROT * role) & 3; return (16 * (p & 1) + (p >> 1)) * XROW; };
  auto ld_part = [&](u32x4 (&p)[9], int i, auto LO, auto N) { const u32 so = grp_so(i); sfor<N.value>([&](auto C) { constexpr int c = LO.value + C.value; ld128_asm<c * 128>(p[c], pa0, X.srd, so); }); };
  auto ld_group = [&](u32x4 (&p)[9], int i) { ld_part(p, i, IC(0), IC(9)); };
  auto xwr = [&](int p) { return ldsb + XBASE + w * 8192 + (8 * p + r8) * 128 + 16 * (s8 ^ (r8 >> 1) ^ (4 * (p & 1))); };
  auto sdst = [&](int i) { return ldsb + SBASE + w * 256 + (8 * ((i + XROT * role) & 3) + r8) * 8; };
  u32 ep0, ep1;
  srd_t srdWS;
  u32 v16, v0, xdst, colswz;
  auto common = [&]() {
    epoch_issue(ep0, ws + WS_EPOCH + tile * 128);
    epoch_issue(ep1, ws + WS_EPOCH + ((tile + 1) & 15) * 128);
    srdWS = mksrd(ws, WS_END);
    v16 = l * 16; v0 = l == 0 ? 0u : OOB;
#pragma unroll
    for (int p = 0; p < 4; ++p) X.vx[p] = BIAS + (tok0 + 32 * w + 16 * (p & 1) + 2 * r8 + (p >> 1)) * XROW + 16 * (s8 ^ (r8 >> 1) ^ (4 * (p & 1))) - 1024 * p;
#pragma unroll
    for (int p = 0; p < 4; ++p) pinw(X.vx[p]);
    X.bx = ldsb + XBASE + w * 8192 + tk * 128 + 16 * (g ^ (tk >> 1));
    xdst = ldsb + XBASE + w * 8192;
    colswz = 32 * ((s8 >> 1) ^ (r8 >> 1)) + 16 * (s8 & 1);
  };
  auto ln_fetch = [&]() {
    u32x2 t0, t1;
    asm volatile("ds_read_b64 %0, %2\n\tds_read_b64 %1, %2 offset:128\n\ts_waitcnt lgkmcnt(0)" : "=&v"(t0), "=&v"(t1) : "v"(ldsb + SBASE + w * 256 + tk * 8) : "memory");
    X.rs[0] = __uint_as_float(t0[0]); X.sh[0] = __uint_as_float(t0[1]); X.rs[1] = __uint_as_float(t1[0]); X.sh[1] = __uint_as_float(t1[1]); };
  if (__builtin_expect(role < 3, 1)) {
    u32x4 pA[9], pB[9];
    ld_group(pA, 0); ld_part(pB, 1, IC(0), IC(NFIRST - 9));
    SB0();
    common();
    const u32 slice = sq * 3 + role;
    const u32 om = (sq + role) & 3;
    const u32 gm[3] = {om == 0 ? 1u : om == 1 ? 2u : 0u, om == 0 ? 2u : om == 1 ? 0u : 1u, om == 3 ? 2u : om};
    srd_t dsrd = mksrd_b(w1, DM * W1ROW);
    u32 dvl = (8 * (r8 >> 2) + (r8 & 3)) * W1ROW + colswz;
    u32 dk[6];
#pragma unroll
    for (int c = 0; c < 6; ++c) { const u32 pi = 6 * w + c; dk[c] = BIAS + (16 * (pi & 1) + 4 * ((pi >> 1) & 1)) * W1ROW + slice * 768 + (pi >> 2) * 128 - 1024 * (c & 3); }
    u32 dso = 0, dd1 = 32 * W1ROW, dd2 = 32 * W1ROW, dwrap = ~0u;
    const u32 dm0lo = ldsb + w * 6144, dm0hi = dm0lo + FNS * FSLOT;
    u32 dm0 = dm0lo;
    auto dma_piece = [&](auto C) { constexpr int c = C.value; dma16<(c & 3) * 1024>(dvl, dsrd, dso + dk[c]); };
    auto dma_adv = [&](auto E) {
      if constexpr (E.value == 0) dso += dd1; else { dso += dd2; dso = dso == dwrap ? 0 : dso; }
      dm0 += FSLOT; dm0 = dm0 == dm0hi ? dm0lo : dm0; };
    auto dma_all = [&](auto E) { setm0(dm0); sfor<4>(dma_piece); setm0(dm0 + 4096); dma_piece(IC(4)); dma_piece(IC(5)); dma_adv(E); };
    constexpr int NVA = 20;
    f32x4 acc[24][2];
    auto zacc = [&](auto LO, auto N) { sfor<N.value>([&](auto I) { constexpr int k = LO.value + I.value; if constexpr (k < 2 * NVA) zero_v(acc[k >> 1][k & 1]); else zero_a(acc[k >> 1][k & 1]); }); };
    zacc(IC(0), IC(48));
    u32 cur[4], nxt[4];
    u32 rdelta = FSLOT, rslot = 0;
    constexpr int LA = 8, NF = 12;
    s16x4 Fl[NF], Fh[NF];
    auto rdhalf = [&](auto A, auto T, const u32 (&bs)[4]) { constexpr int a = A.value;
      if constexpr (T.value == 0) FR<a % NF>::template lo<(a >> 2) * 4096>(Fl[a % NF], bs[a & 3]); else FR<a % NF>::template hi<(a >> 2) * 4096 + 2048>(Fh[a % NF], bs[a & 3]); };
#pragma unroll
    for (int u = 0; u < 4; ++u) cur[u] = ldsb + hh * 1024 + 128 * rr + 32 * (u ^ (rr >> 1)) + 8 * p4;
    const srd_t psrd = mksrd(w2, DFF * XROW);
    u32 pso_ = (slice * 384 + 48 * t7) * XROW + w * 1024;
    const u32 plim_ = (slice * 384 + 48 * t7 + 48) * XROW - 1024;
    ld_part(pB, 1, IC(NFIRST - 9), IC(18 - NFIRST));
    dma_all(IC(0));
    ln_group<15>(pA, sdst(0), xwr(0));
    ld_group(pA, 2);
    ln_group<15>(pB, sdst(1), xwr(1));
    ld_group(pB, 3);
    ln_group<9>(pA, sdst(2), xwr(2));
    dma_all(IC(1));
    __builtin_amdgcn_s_barrier();
    u32x4 B[2], Bn[2], raw[2];
    sfor<LA>([&](auto A) { rdhalf(A, IC(0), cur); rdhalf(A, IC(1), cur); });
    ln_group<6>(pB, sdst(3), xwr(3));
    rd128_v<0>(raw[0], X.bx); rd128_v<2048>(raw[1], X.bx);
    setm0(ldsb + TBASE); dma16<0>(v16, psrd, pso_);
    dma_all(IC(0));
    setm0(ldsb + TBASE); dma16<0>(v16, psrd, pso_);
    STAMP(1);
    ln_fetch();
    epoch_wait(ep0, ep1);
    pinu(raw[0]); pinu(raw[1]);
    B[0] = norm_frag(raw[0], X.rs[0], X.sh[0]); B[1] = norm_frag(raw[1], X.rs[1], X.sh[1]);
    asm volatile("s_nop 1" : "+v"(B[0]), "+v"(B[1]));
    STAMP(2);
    auto stage = [&](auto E, auto NV, auto DMA, auto L8, auto&& MM, auto&& MEM, auto&& EX, auto&& BAR) {
      sfor<48>([&](auto M) { constexpr int m = M.value, a = m >> 1, tb = m & 1;
        if constexpr (m == 32 && NV.value >= 0) { if constexpr (NV.value < 63) waitvm<NV.value>(); __builtin_amdgcn_s_barrier(); BAR(); }
        if constexpr (m % 8 == 0) wl<(NV.value < 0 && m >= 40) ? 0 : (m == 8 ? L8.value : 8)>();
        MM(M);
        if constexpr (a + LA < 24) rdhalf(IC(a + LA), IC(tb), cur); else if constexpr (NV.value >= 0) rdhalf(IC(a + LA - 24), IC(tb), nxt);
        if constexpr (DMA.value && dpos(m) >= 0) dma_piece(IC(dpos(m)));
        MEM(M);
        SB0();
        if constexpr (DMA.value && m == 2) setm0_add<0>(dm0);
        if constexpr (DMA.value && m == 26) setm0_add<4096>(dm0);
        if constexpr (DMA.value && m == 31) dma_adv(IC(E.value ^ 1));
        if constexpr (m == 1) { rslot += FSLOT; rdelta = rslot == FNS * FSLOT ? 0u - (FNS - 1) * FSLOT : FSLOT; rslot = rslot == FNS * FSLOT ? 0 : rslot; }
        if constexpr (m >= 2 && m < 6 && NV.value >= 0) nxt[m - 2] = v_addv(cur[m - 2], rdelta);
        EX(M);
        SB0();
      });
#pragma unroll
      for (int u = 0; u < 4; ++u) cur[u] = nxt[u];
    };
    auto nop = [&](auto) {};
    auto nop0 = [&]() {};
    constexpr auto tbo = [](int m) { return GRAY ? ((m ^ (m >> 1)) & 1) : (m & 1); };
    u32 xs = 0, xso = 256, ba = X.bx ^ 64;
    u32 tlo[2], thi[2];
    for (int it = 0; it < 9; ++it) {
      sfor<2>([&](auto E) { constexpr int e = E.value;
        if constexpr (e == 0) { if (it == 7) X.srd = mksrd(x, 0); }
        if constexpr (e == 1) { if (it == 7) {
            dsrd = mksrd_b(w2, DFF * XROW); dvl = r8 * XROW + colswz;
#pragma unroll
            for (int c = 0; c < 6; ++c) dk[c] = BIAS + (48 * w + 8 * c) * XROW + role * 384 - 1024 * (c & 3);
            dso = slice * 384 * XROW + gm[0] * 128; dd1 = 192 * XROW; dd2 = (gm[1] - gm[0]) * 128 - 192 * XROW;
          } }
        stage(E, IC(e == 0 ? 14 : 18), IC(1), IC(6),
              [&](auto M) { constexpr int a = M.value >> 1, tb = tbo(M.value); if constexpr (a < NVA) FR<a % NF>::vv(acc[a][tb], Fl[a % NF], Fh[a % NF], B[tb]); else FR<a % NF>::av(acc[a][tb], Fl[a % NF], Fh[a % NF], B[tb]); },
              [&](auto M) { constexpr int m = M.value;
                if constexpr (m == 0) rd128_v<0>(raw[0], ba);
                if constexpr (m == 1) rd128_v<2048>(raw[1], ba);
                if constexpr (e == 0 && m >= 34 && m < 46 && (m - 34) % 3 == 0) dma16<((m - 34) / 3) * 1024>(X.vx[(m - 34) / 3], X.srd, xso);
                if constexpr (m == 46) dma16<0>(v16, psrd, pso_); },
              [&](auto M) { constexpr int m = M.value;
                if constexpr (e == 0 && m == 33) setm0_add<0>(xdst + xs);
                if constexpr (e == 0 && m == 44) { xso += 128; xs ^= 4096; }
                if constexpr (m == 45) setm0_add<TBASE>(ldsb);
                if constexpr (m == 47) { pso_ += 4096; pso_ = pso_ > plim_ ? plim_ : pso_; }
                if constexpr (m == 6) ba = v_addv(e == 0 ? X.bx : (X.bx ^ 64), e == 0 ? (xs ^ 4096) : xs);
                if constexpr (m >= 8) norm_op<m - 8>(X, raw, Bn, tlo, thi); },
              nop0);
        B[0] = Bn[0]; B[1] = Bn[1];
        asm volatile("s_nop 0" : "+v"(B[0]), "+v"(B[1]));
      });
    }
    STAMP(3);
    asm volatile("s_nop 7");
    u32x4 H[12][2];
    const u32 hb0 = WS_H + ((tile * 12 + slice) * 4 + w) * 24576;
#pragma unroll
    for (int k = 0; k < 12; ++k)
#pragma unroll
      for (int tb = 0; tb < 2; ++tb) {
        if (2 * k < NVA) { pin(acc[2 * k][tb]); pin(acc[2 * k + 1][tb]); } else { pin_a(acc[2 * k][tb]); pin_a(acc[2 * k + 1][tb]); }
#pragma unroll
        for (int i = 0; i < 4; ++i) { const f32x4 s = acc[2 * k + (i >> 1)][tb];
          H[k][tb][i] = __builtin_bit_cast(u32, __builtin_elementwise_max(__builtin_bit_cast(s16x2, cvtpk(s[2 * (i & 1)], s[2 * (i & 1) + 1])), s16x2{0, 0})); }
        if (k < 3) st128(H[k][tb], srdWS, v16 + ((2 * k + tb) & 3) * 1024, hb0 + (k >> 1) * 4096);
      }
    const u32 tag = (ep0 + 1) << 4;
    const u32 fso = WS_FLAG + (tile * 4 + w) * 128;
    const u32 slB = sq * 3 + (role == 2 ? 0 : role + 1), slC = sq * 3 + (role == 0 ? 2 : role - 1);
    const u32 sbase[3] = {slice * 384 * XROW, slB * 384 * XROW, slC * 384 * XROW};
    const u32 hbB = WS_H + ((tile * 12 + slB) * 4 + w) * 24576, hbC = WS_H + ((tile * 12 + slC) * 4 + w) * 24576;
    const u32 pg0 = WS_P + ((tile * 9 + role * 3) * 32 + w) * 4096;
    const u32 vpoll = l < 16 ? l * 4 : OOB;
    const u32 lim12 = (l == slB || l == slC) ? 13u : ~0u;
    const bool owner = om < 3;
    const u32 pown = pg0 + gm[2] * 131072;
    const u32 sqi[3] = {(om + 2 >= 3 ? om - 1 : om + 2) - role & 3, (om + 1 >= 3 ? om - 2 : om + 1) - role & 3, (3 - role) & 3};
    const u32 ned[3] = {3, 4, 3 + om};
    bool got[4] = {false, false, false, false}, pend[4] = {false, false, false, false};
    f32x4 ac3[3][4][2], acs[4][2];
    u32x4 HB[12][2], HC[12][2], sd[4], SP[4][4], PQ[3][4];
    u32 pw, pvS = OOB, pv0 = OOB, pv1 = OOB;
    s16x8 sel0, sel1;
    auto rdy = [&](auto I) { constexpr int i = I.value;
      if constexpr (i < 3) return __all(l != sqi[i] * 3 + role || pw - (tag + ned[i]) <= 15u - ned[i]);
      else return __all(l < 12 || l >= 16 || pw - (tag + role * 3 + gm[2] + 1) <= 14u - (role * 3 + gm[2])); };
    auto red_f = [&](u32x4 (&P)[4]) { sfor<4>([&](auto U) { mfma_a(ac3[2][U.value][0], sel0, P[U.value]); mfma_a(ac3[2][U.value][1], sel1, P[U.value]); }); };
    auto red_s = [&]() { sfor<16>([&](auto I) { constexpr int q = I.value >> 2, u = I.value & 3; mfma_a(acs[u][0], sel0, SP[q][u]); mfma_a(acs[u][1], sel1, SP[q][u]); }); };
    auto ld_s = [&](auto I) { constexpr int i = I.value; ld128_a<(i & 3) * 1024>(SP[i >> 2][i & 3], pvS, srdWS, pown + (4 + (i >> 2)) * 16384); };
    const srd_t srdX = mksrd_b(x, SEQ * XROW), srdO = mksrd_b(out, SEQ * XROW);
    const u32 ecol = (role * 3 + gm[2]) * 128;
    asm volatile("s_nop 1");
    STAMP(4);
    auto g2stage = [&](auto OO, auto JJ) { constexpr int J = JJ.value; constexpr bool O = OO.value;
      constexpr auto whf = [](int j) { return j < 6 ? 0 : j < 9 ? 1 : j < 12 ? 2 : 1 + (j & 1); };
      constexpr auto gaf = [](int j) { return j < 6 ? j >> 1 : j < 12 ? j % 3 : (j - 12) >> 1; };
      constexpr auto haf = [](int j) { return j < 6 ? j & 1 : j < 12 ? 0 : 1; };
      constexpr int which = whf(J), gam = gaf(J), half = haf(J), Jn = J + 4;
      constexpr int nw = J <= 16 ? f2count(O, J - 2, 30, J, 32) : -1;
      constexpr int nv = J == 4 ? imin(nw, f2after(O, OP_POLL, 0, J, 32)) : J == 8 ? imin(nw, f2after(O, OP_POLL, 1, J, 32)) : (O && J == 13) ? imin(nw, f2after(O, OP_POLL, 2, J, 32)) : nw;
      stage(IC(J & 1), IC(nv), IC(J <= 14), IC(8),
            [&](auto M) { constexpr int m = M.value, a = m >> 1, tb = tbo(m), u = a & 3, ks = a >> 2, kap = 6 * half + ks;
              if constexpr (m % 8 == 0 && J == 6) waitvm<f2after(O, OP_HB, 2 * ks + 1, J, m)>();
              if constexpr (m == 0 && J == 9) waitvm<f2after(O, OP_HC, 11, J, 0)>();
              if constexpr (m == 0 && J == 12) waitvm<f2after(O, OP_HB, 23, J, 0)>();
              if constexpr (m == 0 && J == 13) waitvm<f2after(O, OP_HC, 23, J, 0)>();
              if constexpr (which == 0) { if constexpr (half == 0 && ks == 0) FR<a % NF>::vv0(ac3[gam][u][tb], Fl[a % NF], Fh[a % NF], H[0][tb]); else FR<a % NF>::vv(ac3[gam][u][tb], Fl[a % NF], Fh[a % NF], H[kap][tb]); }
              else if constexpr (which == 1) FR<a % NF>::va(ac3[gam][u][tb], Fl[a % NF], Fh[a % NF], HB[kap][tb]);
              else FR<a % NF>::va(ac3[gam][u][tb], Fl[a % NF], Fh[a % NF], HC[kap][tb]); },
            [&](auto M) { constexpr int m = M.value, op = f2op(O, J, m), i = f2index(O, J, m);
              if constexpr (op == OP_HS) st128(H[3 + (i >> 1)][i & 1], srdWS, v16 + ((i + 2) & 3) * 1024, hb0 + ((i + 6) >> 2) * 4096);
              if constexpr (op == OP_HB) ld128_a<(i & 3) * 1024>(HB[i >> 1][i & 1], v16, srdWS, hbB + (i >> 2) * 4096);
              if constexpr (op == OP_HC) ld128_a<(i & 3) * 1024>(HC[i >> 1][i & 1], v16, srdWS, hbC + (i >> 2) * 4096);
              if constexpr (op == OP_FS) st128(sd[i & 3], srdWS, v16 + (i & 3) * 1024, pg0 + gm[i >> 2] * 131072 + sq * 16384);
              if constexpr (op == OP_SL) ld_s(IC(i));
              if constexpr (op == OP_PL) { constexpr int q = (i & 4) ? 2 : 0; ld128_a<(i & 3) * 1024>(PQ[q][i & 3], (i & 4) ? pv1 : pv0, srdWS, pown + sqi[q] * 16384); }
              if constexpr (op == OP_MISC) dma16<i * 1024>(X.vx[i], srdX, ecol);
              if constexpr (op == OP_POLL) poll_issue(pw, vpoll, srdWS, fso);
              if constexpr (op == OP_FLAG) {
                waitvm<(i == 0 ? f2after(O, OP_HS, 5, J, m) : i == 1 ? f2after(O, OP_HS, 17, J, m) : f2after(O, OP_FS, 4 * (i - 2) + 3, J, m))>();
                st_flag(tag + 1 + i, srdWS, v0, fso + slice * 4); }
              if constexpr ((J == 14 || J == 16) && m == 13) keep4(sd[0], sd[1], sd[2], sd[3]); },
            [&](auto M) { constexpr int m = M.value;
              if constexpr (m == 40 && Jn < 18) dso = sbase[whf(Jn)] + haf(Jn) * 192 * XROW + gm[gaf(Jn)] * 128;
              if constexpr ((J == 14 || J == 16) && m < 8) { constexpr int u = m >> 1, tb = m & 1, gp = (J - 14) >> 1;
                sd[u][2 * tb] = v_cvtf(ac3[gp][u][tb][0], ac3[gp][u][tb][1]); sd[u][2 * tb + 1] = v_cvtf(ac3[gp][u][tb][2], ac3[gp][u][tb][3]); }
              if constexpr (O && J == 12 && m >= 8 && m < 16) zero_v(acs[(m - 8) >> 1][m & 1]);
              if constexpr (O && J == 12 && m == 20) {
                const u32 on = (tk >> 2) == g ? 0x3f80u << (16 * (tk & 1)) : 0u, s0 = (tk & 2) ? 0u : on, s1 = (tk & 2) ? on : 0u;
                sel0 = __builtin_bit_cast(s16x8, u32x4{s0, s1, 0u, 0u}); sel1 = __builtin_bit_cast(s16x8, u32x4{0u, 0u, s0, s1}); pins(sel0); pins(sel1); }
              if constexpr (O && J == 16 && m == 15) setm0_add<0>(xdst);
              if constexpr (O && J == 16 && m == 44) { waitvm<f2after(O, OP_POLL, 3, J, 45)>(); waitvm_v<63>(pw);
                if (pend[3]) red_s();
                pend[3] = false; pend[0] = owner && rdy(IC(0)); pend[2] = owner && rdy(IC(2)); got[0] = pend[0]; got[2] = pend[2]; pv0 = pend[0] ? v16 : OOB; pv1 = pend[2] ? v16 : OOB; } },
            [&]() {
              if constexpr (J == 4) { waitvm_v<63>(pw); if (!__all(pw - (tag + 1) <= lim12 + (lim12 != ~0u))) spin2(vpoll, srdWS, fso, tag + 1, lim12 + (lim12 != ~0u)); }
              if constexpr (J == 8) { waitvm_v<63>(pw); if (!__all(pw - (tag + 2) <= lim12)) spin2(vpoll, srdWS, fso, tag + 2, lim12); }
              if constexpr (O && J == 13) { waitvm_v<63>(pw); pend[3] = owner && rdy(IC(3)); got[3] = pend[3]; pvS = pend[3] ? v16 : OOB; } });
    };
    sfor<18>([&](auto JJ) { g2stage(std::true_type{}, JJ); if constexpr (JJ.value == 5) STAMP(5); });
    STAMP(6);
    nop8(ac3[2]);
    if (!owner) {
      sfor<8>([&](auto I) { constexpr int u = I.value >> 1, tb = I.value & 1;
        sd[u][2 * tb] = v_cvtf(ac3[2][u][tb][0], ac3[2][u][tb][1]); sd[u][2 * tb + 1] = v_cvtf(ac3[2][u][tb][2], ac3[2][u][tb][3]); });
      asm volatile("s_nop 0");
      sfor<4>([&](auto U) { st128(sd[U.value], srdWS, v16 + U.value * 1024, pown + sq * 16384); });
      waitvm<0>();
      st_flag(tag + 5, srdWS, v0, fso + slice * 4);
      STAMP(7);
      STAMP_FLUSH();
      return;
    }
    sfor<4>([&](auto U) { undef_a(PQ[1][U.value]); });
#pragma unroll 1
    for (u32 it = 0; it < SPIN_MAX; ++it) {
      waitvm_v<0>(pw);
      if (pend[0]) red_f(PQ[0]); if (pend[1]) red_f(PQ[1]); if (pend[2]) red_f(PQ[2]); if (pend[3]) red_s();
      if (got[0] && got[1] && got[2] && got[3]) break;
      sfor<3>([&](auto I) { constexpr int i = I.value; pend[i] = !got[i] && rdy(I);
        if (pend[i]) { got[i] = true; sfor<4>([&](auto U) { ld128_a<U.value * 1024>(PQ[i][U.value], v16, srdWS, pown + sqi[i] * 16384); }); } });
      pend[3] = !got[3] && rdy(IC(3));
      if (pend[3]) { got[3] = true; pvS = v16; sfor<16>(ld_s); }
      if (!(got[0] && got[1] && got[2] && got[3])) poll_issue(pw, vpoll, srdWS, fso);
    }
    asm volatile("s_nop 7");
    const u32 ia0 = xdst + tk * 128 + 16 * (hh ^ (tk >> 1)) + 8 * (g & 1);
#pragma unroll
    for (int u = 0; u < 4; ++u)
#pragma unroll
      for (int tb = 0; tb < 2; ++tb) {
        pin(ac3[2][u][tb]); pin(acs[u][tb]);
        const u32 ia = (ia0 ^ (32 * u)) + tb * 2048;
        const u32x2 xv = rd64(ia);
        u32x2 o;
#pragma unroll
        for (int d = 0; d < 2; ++d) {
          const f32x2 f = {ac3[2][u][tb][2 * d], ac3[2][u][tb][2 * d + 1]}, sv = {acs[u][tb][2 * d], acs[u][tb][2 * d + 1]};
          const f32x2 t = f * -1.4426950408889634f;
          const f32x2 e = f32x2{__builtin_amdgcn_exp2f(t[0]), __builtin_amdgcn_exp2f(t[1])} + 1.f;
          const f32x2 gt = f * f32x2{__builtin_amdgcn_rcpf(e[0]), __builtin_amdgcn_rcpf(e[1])};
          const f32x2 r = __builtin_elementwise_fma(sv, gt, f32x2{bflo(xv[d]), bfhi(xv[d])});
          o[d] = cvtpk(r[0], r[1]);
        }
        wr64(ia, o);
      }
#pragma unroll
    for (int c = 0; c < 4; ++c) __builtin_amdgcn_raw_buffer_store_b128(rd128(xdst + c * 1024 + v16), srdO, X.vx[c] + c * 1024, ecol, 0);
    if (role == 0 && om == 0 && w == 0) st_flag(ep0 + 1, srdWS, v0, WS_EPOCH + tile * 128);
    STAMP(7);
    STAMP_FLUSH();
  } else {
    u32x4 pA[9], pB[9];
    ld_group(pA, 0); ld_part(pB, 1, IC(0), IC(NFIRST - 9));
    SB0();
    common();
    srd_t dsrd = mksrd_b(wqkv, DM * WQROW);
    u32 dv[7];
#pragma unroll
    for (int c = 0; c < 7; ++c) { u32 pi = 7 * w + c; pi = pi > 26 ? 26 : pi;
      const u32 seg = pi / 9, idx = (pi - seg * 9) * 64 + l, rho = idx / 18, ch = idx - rho * 18;
      const u32 krow = 16 * ((rho >> 3) & 1) + 4 * (rho >> 4) + 8 * ((rho >> 2) & 1) + (rho & 3);
      dv[c] = BIAS + krow * WQROW + seg * 1152 + sq * 288 + ch * 16 - 1024 * (c & 3); }
    u32 dso = 0;
    const u32 dm0lo = ldsb + w * 7168, dm0hi = dm0lo + ANS * ASLOT;
    u32 dm0 = dm0lo;
    auto dma_piece = [&](auto C) { constexpr int c = C.value; dma16<(c & 3) * 1024>(dv[c], dsrd, dso); };
    auto dma_adv = [&]() { dso += 32 * WQROW; dm0 += ASLOT; dm0 = dm0 == dm0hi ? dm0lo : dm0; };
    auto dma_all = [&]() { setm0(dm0); sfor<4>(dma_piece); setm0(dm0 + 4096); dma_piece(IC(4)); dma_piece(IC(5)); dma_piece(IC(6)); dma_adv(); };
    f32x4 acc[27][2];
    auto zacc = [&](auto LO, auto N) { sfor<N.value>([&](auto I) { constexpr int k = LO.value + I.value; if constexpr (k < 18) zero_a(acc[k >> 1][k & 1]); else zero_v(acc[k >> 1][k & 1]); }); };
    zacc(IC(0), IC(54));
    u32 cur = ldsb + (8 * hh + rr) * 288 + 8 * p4, nxt = cur, rslot = 0, rdelta = ASLOT;
    constexpr int LA = 6;
    s16x4 Fl[9], Fh[9];
    auto rdhalf = [&](auto A, auto T, u32 bs) { constexpr int a = A.value, off = (a / 9) * 9216 + (a % 9) * 32;
      if constexpr (T.value == 0) FR<a % 9>::template lo<off>(Fl[a % 9], bs); else FR<a % 9>::template hi<off + 16 * 288>(Fh[a % 9], bs); };
    const srd_t psrd = mksrd(wqkv, DM * WQROW);
    const u32 pf0 = l < 48 ? (4 * t7 + l / 12) * WQROW + ((sq * 288) & ~127u) + (l % 12) * 32 : OOB;
    const u32 pfl = w < 3 ? pf0 + w * 1152 : l * 32;
    const srd_t psrdl = w < 3 ? psrd : mksrd(wo, DM * XROW);
    const int plo_ = w < 3 ? 0 : (144 * sq + 18 * t7) * XROW, phi_ = w < 3 ? 17 * 32 * WQROW : plo_ + 18 * XROW - 2048, pst_ = w < 3 ? 32 * WQROW : 2048;
    int pso_ = w < 3 ? 6 * 32 * WQROW : plo_ - 7 * 2048;
    ld_part(pB, 1, IC(NFIRST - 9), IC(18 - NFIRST));
    dma_all();
    ln_group<16>(pA, sdst(0), xwr(0));
    ld_group(pA, 2);
    ln_group<16>(pB, sdst(1), xwr(1));
    ld_group(pB, 3);
    ln_group<9>(pA, sdst(2), xwr(2));
    dma_all();
    __builtin_amdgcn_s_barrier();
    u32x4 B[2], Bn[2], raw[2];
    sfor<LA>([&](auto A) { rdhalf(A, IC(0), cur); rdhalf(A, IC(1), cur); });
    ln_group<7>(pB, sdst(3), xwr(3));
    rd128_v<0>(raw[0], X.bx); rd128_v<2048>(raw[1], X.bx);
    setm0(ldsb + TBASE); sfor<3>([&](auto S) { touch(pf0, psrd, (2 + w) * 32 * WQROW + S.value * 1152); });
    STAMP(1);
    ln_fetch();
    epoch_wait(ep0, ep1);
    pinu(raw[0]); pinu(raw[1]);
    B[0] = norm_frag(raw[0], X.rs[0], X.sh[0]); B[1] = norm_frag(raw[1], X.rs[1], X.sh[1]);
    asm volatile("s_nop 1" : "+v"(B[0]), "+v"(B[1]));
    STAMP(2);
    u32 xs = 0, xso = 256, ba = X.bx ^ 64;
    u32 tlo[2], thi[2];
    for (int it = 0; it < 9; ++it) {
      if (it == 7) X.srd = mksrd(x, 0);
      if (it == 8) dsrd = mksrd(wqkv, 0);
      sfor<2>([&](auto E) { constexpr int e = E.value;
        sfor<54>([&](auto M) { constexpr int m = M.value, a = m >> 1, tb = m & 1, tq = GRAY ? ((m ^ (m >> 1)) & 1) : tb;
          if constexpr (m == 42) { waitvm<e == 0 ? 8 : 12>(); __builtin_amdgcn_s_barrier(); }
          if constexpr (m % 6 == 0) wl<6>();
          if constexpr (a < 9) FR<a % 9>::av(acc[a][tq], Fl[a % 9], Fh[a % 9], B[tq]); else FR<a % 9>::vv(acc[a][tq], Fl[a % 9], Fh[a % 9], B[tq]);
          if constexpr (a + LA < 27) rdhalf(IC(a + LA), IC(tb), cur); else rdhalf(IC(a + LA - 27), IC(tb), nxt);
          if constexpr (m % 6 == 3 && m < 42) dma_piece(IC(m / 6));
          if constexpr (m == 0) rd128_v<0>(raw[0], ba);
          if constexpr (m == 1) rd128_v<2048>(raw[1], ba);
          if constexpr (e == 0 && m >= 44 && m < 52 && (m & 1) == 0) dma16<((m - 44) / 2) * 1024>(X.vx[(m - 44) / 2], X.srd, xso);
          if constexpr (m == 52) touch(pfl, psrdl, (u32)(pso_ < plo_ ? plo_ : pso_));
          SB0();
          if constexpr (m == 2) setm0_add<0>(dm0);
          if constexpr (m == 26) setm0_add<4096>(dm0);
          if constexpr (m == 40) dma_adv();
          if constexpr (e == 0 && m == 43) setm0_add<0>(xdst + xs);
          if constexpr (e == 0 && m == 53) { xso += 128; xs ^= 4096; }
          if constexpr (m == 51) setm0_add<TBASE>(ldsb);
          if constexpr (m == 53) { pso_ += pst_; pso_ = pso_ > phi_ ? phi_ : pso_; }
          if constexpr (m == 1) { rslot += ASLOT; rdelta = rslot == ANS * ASLOT ? 0u - (ANS - 1) * ASLOT : ASLOT; rslot = rslot == ANS * ASLOT ? 0 : rslot; }
          if constexpr (m == 4) nxt = v_addv(cur, rdelta);
          if constexpr (m == 6) ba = v_addv(e == 0 ? X.bx : (X.bx ^ 64), e == 0 ? (xs ^ 4096) : xs);
          if constexpr (m >= 12 && m < 52) norm_op<m - 12>(X, raw, Bn, tlo, thi);
          SB0();
        });
        cur = nxt; B[0] = Bn[0]; B[1] = Bn[1];
      });
    }
    STAMP(3);
    asm volatile("s_nop 7\n\ts_waitcnt vmcnt(0) lgkmcnt(0)" ::: "memory");
    u32x4 Kp[9], Kq[9], Vp[9];
    u32x2 Qp[9][2];
    const bool hsend = w == 3 && tile < 15;
    const u32 hsn = WS_HALO + ((tile + 1) * 4 + sq) * 18432, hfn = WS_HFLAG + ((tile + 1) * 4 + sq) * 128;
#pragma unroll
    for (int b = 0; b < 9; ++b) { pin(acc[9 + b][0]); pin(acc[9 + b][1]);
      Kp[b] = u32x4{cvtpk(acc[9 + b][0][0], acc[9 + b][0][1]), cvtpk(acc[9 + b][0][2], acc[9 + b][0][3]), cvtpk(acc[9 + b][1][0], acc[9 + b][1][1]), cvtpk(acc[9 + b][1][2], acc[9 + b][1][3])}; }
    if (hsend) {
#pragma unroll
      for (int b = 0; b < 9; ++b) st128(Kp[b], srdWS, v16, hsn + b * 1024);
    }
#pragma unroll
    for (int b = 0; b < 9; ++b) { pin(acc[18 + b][0]); pin(acc[18 + b][1]);
      Vp[b] = u32x4{cvtpk(acc[18 + b][0][0], acc[18 + b][0][1]), cvtpk(acc[18 + b][0][2], acc[18 + b][0][3]), cvtpk(acc[18 + b][1][0], acc[18 + b][1][1]), cvtpk(acc[18 + b][1][2], acc[18 + b][1][3])}; }
    __builtin_amdgcn_s_barrier();
    const srd_t wsrd = mksrd_b(wo, DM * XROW);
    const u32 wvl = r8 * XROW + colswz;
    constexpr auto wslot = [](int G) { return G < 3 ? 57344 + G * 20480 : G < 5 ? (G - 3) * 20480 : G == 5 ? (int)XBASE : 57344 + (G - 6) * 20480; };
    u32 wk[5];
#pragma unroll
    for (int c = 0; c < 5; ++c) { u32 pi = 5 * w + c; pi = pi > 17 ? 17 : pi; wk[c] = BIAS + (144 * sq + 8 * pi) * XROW - 1024 * (c & 3); }
    const u32 wm0 = ldsb + w * 5120;
    auto wo_piece = [&](auto GG, auto C) { constexpr int G = GG.value, c = C.value;
      if constexpr ((c & 3) == 0) { setm0_add<wslot(G) + c * 1024>(wm0); asm volatile("s_nop 0"); }
      dma16<(c & 3) * 1024>(wvl, wsrd, wk[c] + G * 128); };
    auto wo_dma = [&](auto GG) { sfor<5>([&](auto C) { wo_piece(GG, C); }); };
    const u32 vw = ldsb + (16 * (w + 1) + tk) * VROW + 8 * g;
    auto wr_v = [&]() {
#pragma unroll
      for (int b = 0; b < 9; ++b) { wr64(vw + 32 * b, u32x2{Vp[b][0], Vp[b][1]}); wr64(vw + 80 * VROW + 32 * b, u32x2{Vp[b][2], Vp[b][3]}); } };
    if (w < 3) {
      wr_v();
#pragma unroll
      for (int b = 0; b < 9; ++b) wr128(ldsb + XBASE + w * 9216 + b * 1024 + v16, Kp[b]);
      waitlgkm<0>();
    } else if (hsend) {
      waitvm<0>(); st_flag(((ep1 + 1) << 4) + 1, srdWS, v0, hfn);
#pragma unroll
      for (int b = 0; b < 9; ++b) st128(Vp[b], srdWS, v16, hsn + 9216 + b * 1024);
    }
    __builtin_amdgcn_s_barrier();
    if (w == 3) wr_v();
#pragma unroll
    for (int b = 0; b < 9; ++b)
#pragma unroll
      for (int tb = 0; tb < 2; ++tb) { pin_a(acc[b][tb]); Qp[b][tb] = u32x2{cvtpk(acc[b][tb][0], acc[b][tb][1]), cvtpk(acc[b][tb][2], acc[b][tb][3])}; }
#pragma unroll
    for (int b = 0; b < 9; ++b) pinu2(Qp[b][0], Qp[b][1]);
    u32x4 Vh[9];
    const u32 hv = tile > 0 ? v16 : OOB;
    const u32 hso = WS_HALO + (tile * 4 + sq) * 18432, hfo = WS_HFLAG + (tile * 4 + sq) * 128;
    if (hsend) { waitvm<0>(); st_flag(((ep1 + 1) << 4) + 2, srdWS, v0, hfn); }
    if (w > 0) {
#pragma unroll
      for (int b = 0; b < 9; ++b) Kq[b] = rd128(ldsb + XBASE + (w - 1) * 9216 + b * 1024 + v16);
    } else {
      if (tile > 0) spin(v0, srdWS, hfo, (ep0 + 1) << 4, l == 0 ? 1u : 0u);
#pragma unroll
      for (int b = 0; b < 9; ++b) ld128_vs(Kq[b], hv, srdWS, hso + b * 1024);
    }
    wo_dma(IC(0)); wo_dma(IC(1)); wo_dma(IC(2));
    wait9<15>(Kq);
    STAMP(4);
    f32x4 cmc, cmp; u32 mcur[2];
    {
      const bool hasprev = tile > 0 || w > 0;
      u32 mb[4];
#pragma unroll
      for (int r = 0; r < 4; ++r) { const int i = 4 * g + r, q = tk; const bool vc = i <= q && i + 7 >= q, vp = hasprev && i >= q + 9;
        cmc[r] = vc ? 0.f : -1e30f; cmp[r] = vp ? 0.f : -1e30f; mb[r] = vc ? 0xffffu : 0u; }
      mcur[0] = mb[0] | (mb[1] << 16); mcur[1] = mb[2] | (mb[3] << 16);
    }
    s16x8 ones = {0x3f80, 0x3f80, 0x3f80, 0x3f80, 0x3f80, 0x3f80, 0x3f80, 0x3f80};
    const float sc = 0.23570226039551584f * 1.4426950408889634f;
    const u32 vr = ldsb + (16 * w + 4 * g + q4) * VROW + 8 * p4;
    f32x4 Sc[2][8], Sp[2][8], Ol[2][8], Oh[2][8], Sm[2][8];
    u32x4 Pf[2][8];
    s16x8 Vt[2][9];
    u32x2 qh[8], ql[8], Ob[2][9];
    auto qmask = [&](auto TB) { constexpr int tb = TB.value;
#pragma unroll
      for (int h = 0; h < 8; ++h) {
#pragma unroll
        for (int d = 0; d < 2; ++d) { qh[h][d] = (2 * g + d >= (u32)h) ? Qp[h][tb][d] : 0u; ql[h][d] = (2 * g + d <= (u32)h) ? Qp[h + 1][tb][d] : 0u; }
        pinu2(qh[h], ql[h]); }
      asm volatile("s_nop 1"); };
    auto phA = [&](auto TB, auto HP, auto J) { constexpr int tb = TB.value, j = J.value, h = 2 * HP.value + ((j >> 1) & 1);
      if constexpr (j < 4) { if constexpr (j & 1) mfma16_c(Sp[tb][h], u32x2{Kq[h][2 * tb], Kq[h][2 * tb + 1]}, qh[h], cmp); else mfma16_c(Sc[tb][h], u32x2{Kp[h][2 * tb], Kp[h][2 * tb + 1]}, qh[h], cmc); }
      else { if constexpr (j & 1) mfma16_v(Sp[tb][h], u32x2{Kq[h + 1][2 * tb], Kq[h + 1][2 * tb + 1]}, ql[h]); else mfma16_v(Sc[tb][h], u32x2{Kp[h + 1][2 * tb], Kp[h + 1][2 * tb + 1]}, ql[h]); } };
    f32x4 val[2]; float mx[2];
    auto phB = [&](auto TB, auto HP, auto I) { constexpr int tb = TB.value, i = I.value;
#pragma unroll
      for (int e = 0; e < 2; ++e) { const int h = 2 * HP.value + e;
        if constexpr (i == 0) { pin(Sc[tb][h]); pin(Sp[tb][h]);
#pragma unroll
          for (int r = 0; r < 4; ++r) val[e][r] = fmax2(Sc[tb][h][r], Sp[tb][h][r]);
          mx[e] = fmax2(fmax2(val[e][0], val[e][1]), fmax2(val[e][2], val[e][3])); }
        if constexpr (i == 1) {
          { auto s = __builtin_amdgcn_permlane16_swap(__float_as_uint(mx[e]), __float_as_uint(mx[e]), false, false); mx[e] = fmax2(__uint_as_float(s[0]), __uint_as_float(s[1])); }
          { auto s = __builtin_amdgcn_permlane32_swap(__float_as_uint(mx[e]), __float_as_uint(mx[e]), false, false); mx[e] = fmax2(__uint_as_float(s[0]), __uint_as_float(s[1])); }
          mx[e] = -mx[e] * sc; }
        if constexpr (i == 2) {
#pragma unroll
          for (int r = 0; r < 4; ++r) val[e][r] = __builtin_amdgcn_exp2f(__builtin_fmaf(val[e][r], sc, mx[e])); }
        if constexpr (i == 3) { const u32 k0 = cvtpk(val[e][0], val[e][1]), k1 = cvtpk(val[e][2], val[e][3]);
          Pf[tb][h] = u32x4{k0 & ~mcur[0], k1 & ~mcur[1], k0 & mcur[0], k1 & mcur[1]}; } }
      if constexpr (i < 2) pin2(mx[0], mx[1]);
      if constexpr (i == 2) { pin(val[0]); pin(val[1]); }
      if constexpr (i == 3) { pinu(Pf[tb][2 * HP.value]); pinu(Pf[tb][2 * HP.value + 1]); } };
    auto phC = [&](auto TB, auto HP, auto J) { constexpr int tb = TB.value, j = J.value, h = 2 * HP.value + j / 3, i = j % 3;
      if constexpr (j < 6) {
        if constexpr (i == 0) mfma_v0(Sm[tb][h], ones, Pf[tb][h]);
        if constexpr (i == 1) mfma_v0(Ol[tb][h], Vt[tb][h], Pf[tb][h]);
        if constexpr (i == 2) mfma_v0(Oh[tb][h], Vt[tb][h + 1], Pf[tb][h]); } };
    auto phD = [&](auto TB, auto HD) { constexpr int tb = TB.value, h = HD.value;
      pin(Sm[tb][h]); pin(Ol[tb][h]); pin(Oh[tb][h]);
      const float inv = __builtin_amdgcn_rcpf(Sm[tb][h][0]);
#pragma unroll
      for (int d = 0; d < 2; ++d) { const u32 lo = cvtpk(Ol[tb][h][2 * d] * inv, Ol[tb][h][2 * d + 1] * inv);
        if (h == 0) Ob[tb][0][d] = lo; else Ob[tb][h][d] = (2 * g + d < (u32)h) ? Ob[tb][h][d] : lo;
        Ob[tb][h + 1][d] = cvtpk(Oh[tb][h][2 * d] * inv, Oh[tb][h][2 * d + 1] * inv); }
      pinu2(Ob[tb][h], Ob[tb][h + 1]); };
    auto ld_vt = [&](auto TB) { constexpr int tb = TB.value;
      asm volatile("" ::: "memory");
#pragma unroll
      for (int b = 0; b < 9; ++b) Vt[tb][b] = cat(trrd(vr + tb * 80 * VROW + 32 * b), trrd(vr + (tb * 80 + 16) * VROW + 32 * b)); };
    asm volatile("" : "+v"(cmc), "+v"(cmp), "+v"(ones));
    qmask(IC(0));
    sfor<32>([&](auto J) { phA(IC(0), IC(J.value >> 3), IC(J.value & 7)); });
    qmask(IC(1));
#pragma unroll
    for (int b = 0; b < 9; ++b) undef_a(Vh[b]);
    if (w == 0) {
      if (tile > 0) spin(v0, srdWS, hfo, (ep0 + 1) << 4, l == 0 ? 2u : 0u);
#pragma unroll
      for (int b = 0; b < 9; ++b) ld128_a<0>(Vh[b], hv, srdWS, hso + 9216 + b * 1024);
    }
    asm volatile("s_nop 7");
    sfor<16>([&](auto J) { constexpr int hp = J.value >> 2, i = J.value & 3; phA(IC(1), IC(hp), IC(2 * i)); phA(IC(1), IC(hp), IC(2 * i + 1)); phB(IC(0), IC(hp), IC(i)); });
    if (w == 0) {
      waitvm<0>();
#pragma unroll
      for (int b = 0; b < 9; ++b) { wr64_a(vw - 16 * VROW + 32 * b, Vh[b]); }
    }
    ld_vt(IC(0));
    asm volatile("s_nop 7" :: "v"(cmc), "v"(cmp));
    sfor<16>([&](auto J) { constexpr int hp = J.value >> 2, i = J.value & 3; phC(IC(0), IC(hp), IC(2 * i)); phC(IC(0), IC(hp), IC(2 * i + 1)); phB(IC(1), IC(hp), IC(i)); });
    ld_vt(IC(1));
    asm volatile("s_nop 7");
    sfor<8>([&](auto J) { constexpr int h = J.value; phC(IC(1), IC(h >> 1), IC(3 * (h & 1))); phC(IC(1), IC(h >> 1), IC(3 * (h & 1) + 1)); phC(IC(1), IC(h >> 1), IC(3 * (h & 1) + 2)); phD(IC(0), J); });
    asm volatile("s_nop 7");
    sfor<8>([&](auto J) { phD(IC(1), J); });
    STAMP(5);
    asm volatile("s_waitcnt vmcnt(0) lgkmcnt(0)" ::: "memory");
    __builtin_amdgcn_s_barrier();
    u32x4 OB[4][2];
#pragma unroll
    for (int j = 0; j < 4; ++j)
#pragma unroll
      for (int tb = 0; tb < 2; ++tb) { OB[j][tb] = u32x4{Ob[tb][2 * j][0], Ob[tb][2 * j][1], Ob[tb][2 * j + 1][0], Ob[tb][2 * j + 1][1]}; pinu(OB[j][tb]); }
    pinu2(Ob[0][8], Ob[1][8]);
    u32 wb[3][4];
#pragma unroll
    for (int u = 0; u < 4; ++u) { wb[0][u] = ldsb + hh * 1024 + 128 * rr + 32 * (u ^ (rr >> 1)) + 8 * p4; wb[1][u] = wb[0][u] + 57344; wb[2][u] = wb[0][u] + XBASE; }
    const u32 tag = (ep0 + 1) << 4, fso = WS_FLAG + (tile * 4 + w) * 128 + (12 + sq) * 4;
    const u32 sbase = WS_P + ((tile * 9 * 8 + 4 + sq) * 4 + w) * 4096;
    f32x4 as[4][2];
    u32x4 sd[4];
    s16x4 Wl[12], Wh[12];
    auto wrd = [&](auto FI, auto T) { constexpr int f = FI.value, G = f / 20, k = (f % 20) >> 2, u = f & 3;
      if constexpr (G < 9) { constexpr int so = wslot(G), bs = so >= (int)XBASE ? 2 : so >= 57344 ? 1 : 0, rel = so - (bs == 2 ? (int)XBASE : bs == 1 ? 57344 : 0);
        if constexpr (T.value == 0) FR<f % 12>::template lo<rel + k * 4096>(Wl[f % 12], wb[bs][u]);
        else if constexpr (k < 4) FR<f % 12>::template hi<rel + k * 4096 + 2048>(Wh[f % 12], wb[bs][u]); } };
    auto cvt_s = [&](auto I) { constexpr int u = I.value >> 1, tb = I.value & 1; sd[u][2 * tb] = v_cvtf(as[u][tb][0], as[u][tb][1]); sd[u][2 * tb + 1] = v_cvtf(as[u][tb][2], as[u][tb][3]); };
    sfor<8>([&](auto F) { wrd(F, IC(0)); wrd(F, IC(1)); });
    sfor<9>([&](auto GG) { constexpr int G = GG.value;
      if constexpr (G >= 1) cvt_s(IC(0));
      sfor<40>([&](auto J) { constexpr int j = J.value, k = j >> 3, u = (j >> 1) & 3, tb = j & 1, f = 20 * G + (j >> 1);
        if constexpr (j == 30 && G >= 4 && G != 5) { waitvm<4>(); st_flag(tag + G - 1, srdWS, v0, fso); }
        if constexpr (j == 24 && G == 2) waitvm<16>();
        if constexpr (j == 24 && G == 5) { waitvm<3>(); st_flag(tag + 4, srdWS, v0, fso); }
        if constexpr ((j == 0 && G >= 1 && G <= 3) || (j == 24 && (G == 2 || G == 5))) __builtin_amdgcn_s_barrier();
        if constexpr (j % 8 == 0) wl<(G == 8 && k == 4) ? 0 : (k == 3 ? 4 : 8)>();
        if constexpr (k == 0) FR<f % 12>::vv0(as[u][tb], Wl[f % 12], Wh[f % 12], OB[0][tb]);
        else if constexpr (k < 4) FR<f % 12>::vv(as[u][tb], Wl[f % 12], Wh[f % 12], OB[k][tb]);
        else FR<f % 12>::k16(as[u][tb], Wl[f % 12], Ob[tb][8]);
        wrd(IC(f + 8), IC(tb));
        if constexpr (G == 0 && j < 30 && (j & 1)) wo_piece(IC(3 + j / 10), IC((j % 10) >> 1));
        if constexpr (G >= 1 && G <= 3 && j % 6 == 3 && j < 30) wo_piece(IC(G + 5), IC(j / 6));
        if constexpr (G >= 1 && j >= 6 && j <= 24 && j % 6 == 0) st128(sd[j / 6 - 1], srdWS, v16 + (j / 6 - 1) * 1024, sbase + (G - 1) * 131072);
        if constexpr (G >= 1 && j == 26) keep4(sd[0], sd[1], sd[2], sd[3]);
        SB0();
        if constexpr (G >= 1 && j + 1 < 8) cvt_s(IC(j + 1));
        SB0();
      });
    });
    asm volatile("s_nop 7");
    sfor<8>(cvt_s);
    asm volatile("s_nop 0");
    sfor<4>([&](auto U) { st128(sd[U.value], srdWS, v16 + U.value * 1024, sbase + 8 * 131072); });
    waitvm<0>();
    st_flag(tag + 9, srdWS, v0, fso);
    STAMP(6); STAMP(7);
    STAMP_FLUSH();
  }
}

extern "C" void kernel_launch(void* const* d_in, const int* in_sizes, int n_in, void* d_out, int out_size, void* d_ws, size_t ws_size, hipStream_t stream) {
  (void)hipFuncSetAttribute((const void*)fused, hipFuncAttributeMaxDynamicSharedMemorySize, LDS_BYTES);
  fused<<<256, 256, LDS_BYTES, stream>>>((const u16*)d_in[0], (const u16*)d_in[1], (const u16*)d_in[2], (const u16*)d_in[4], (const u16*)d_in[5], (u16*)d_out, (char*)d_ws);
}
```

```cpp
#include <hip/hip_runtime.h>
#include <utility>
#include <type_traits>
#define XROT 0
#define ABL 0
#define NFIRST 15
#define PAUX "sc1"
#define GRAY 0
typedef unsigned short u16;
typedef unsigned u32;
typedef short s16x2 __attribute__((ext_vector_type(2)));
typedef short s16x4 __attribute__((ext_vector_type(4)));
typedef short s16x8 __attribute__((ext_vector_type(8)));
typedef __bf16 bf16x8 __attribute__((ext_vector_type(8)));
typedef __bf16 bf16x2 __attribute__((ext_vector_type(2)));
typedef float f32x2 __attribute__((ext_vector_type(2)));
typedef float f32x4 __attribute__((ext_vector_type(4)));
typedef u32 u32x2 __attribute__((ext_vector_type(2)));
typedef u32 u32x4 __attribute__((ext_vector_type(4)));
#define LDS3 __attribute__((address_space(3)))
#define DI __device__ __forceinline__
#define SB0() __builtin_amdgcn_sched_barrier(0)
#define IC(x) std::integral_constant<int, (x)>{}
template <int... Is, class F> DI void sfor_impl(std::integer_sequence<int, Is...>, F&& f) { (f(std::integral_constant<int, Is>{}), ...); }
template <int N, class F> DI void sfor(F&& f) { sfor_impl(std::make_integer_sequence<int, N>{}, f); }
typedef __amdgpu_buffer_rsrc_t srd_t;

constexpr int SEQ = 2048, DM = 576, DFF = 4608;
constexpr u32 XROW = 1152, W1ROW = 9216, WQROW = 3456, BIAS = 4096, OOB = 0x80000000u;
constexpr u32 WS_EPOCH = 0;
constexpr u32 WS_FLAG = 4096;
constexpr u32 WS_HFLAG = 16384;
constexpr u32 WS_HALO = 32768;
constexpr u32 WS_P = 2u << 20;
constexpr u32 WS_H = 21u << 20;
constexpr u32 WS_END = WS_H + 16u * 12 * 4 * 24576;
constexpr u32 WS_STAMP = 64u << 20;
constexpr u32 FSLOT = 24576, FNS = 5, ASLOT = 28672, ANS = 4, XBASE = 122880, TBASE = 155648, SBASE = 156672, LDS_BYTES = 157696;
constexpr u32 VROW = 296;
constexpr u32 SPIN_MAX = 1u << 16;

DI srd_t mksrd(const void* p, u32 n) { return __builtin_amdgcn_make_buffer_rsrc((void*)p, (short)0, (int)n, 0x00020000); }
DI srd_t mksrd_b(const void* p, u32 n) { return mksrd((const char*)p - BIAS, n + BIAS); }
DI void setm0(u32 v) { asm volatile("s_mov_b32 m0, %0\n\ts_nop 0" :: "s"(v) : "memory"); }
template <int B> DI void setm0_add(u32 a) { asm volatile("s_add_u32 m0, %0, %1" :: "s"(a), "i"(B) : "memory", "scc"); }
template <int OFF> DI void dma16(u32 voff, srd_t srd, u32 soff) {
  asm volatile("buffer_load_dwordx4 %0, %1, %2 offen offset:%3 lds" :: "v"(voff), "s"(srd), "s"(soff), "i"(OFF) : "memory");
}
template <int OFF> DI void ld128_asm(u32x4& d, u32 voff, srd_t srd, u32 soff) {
  asm volatile("buffer_load_dwordx4 %0, %1, %2, %3 offen offset:%4" : "=v"(d) : "v"(voff), "s"(srd), "s"(soff), "i"(OFF) : "memory");
}
DI void touch(u32 voff, srd_t srd, u32 soff) { if (ABL & 1) voff = OOB; asm volatile("buffer_load_dword %0, %1, %2 offen lds" :: "v"(voff), "s"(srd), "s"(soff) : "memory"); }
DI void poll_issue(u32& d, u32 voff, srd_t srd, u32 soff) { asm volatile("buffer_load_dword %0, %1, %2, %3 offen sc1" : "=v"(d) : "v"(voff), "s"(srd), "s"(soff) : "memory"); }
template <int N> DI void waitvm() { asm volatile("s_waitcnt vmcnt(%0)" :: "i"(N) : "memory"); }
template <int N> DI void waitvm_v(u32& d) { asm volatile("s_waitcnt vmcnt(%1)" : "+v"(d) : "i"(N) : "memory"); }
template <int N> DI void wait9(u32x4 (&p)[9]) {
  asm volatile("s_waitcnt vmcnt(%9)" : "+v"(p[0]), "+v"(p[1]), "+v"(p[2]), "+v"(p[3]), "+v"(p[4]), "+v"(p[5]), "+v"(p[6]), "+v"(p[7]), "+v"(p[8]) : "i"(N) : "memory");
}
template <int N> DI void waitlgkm() { __builtin_amdgcn_s_waitcnt(0xC07F | (N << 8)); }
DI s16x4 trrd(u32 a) { return __builtin_amdgcn_ds_read_tr16_b64_v4i16((LDS3 s16x4*)(size_t)a); }
DI u32x4 rd128(u32 a) { return *(LDS3 u32x4*)(size_t)a; }
DI void wr128(u32 a, u32x4 v) { *(LDS3 u32x4*)(size_t)a = v; }
DI void wr64(u32 a, u32x2 v) { *(LDS3 u32x2*)(size_t)a = v; }
DI u32 cvtpk(float lo, float hi) { f32x2 v = {lo, hi}; return __builtin_bit_cast(u32, __builtin_convertvector(v, bf16x2)); }
DI float bflo(u32 d) { return __uint_as_float(d << 16); }
DI float bfhi(u32 d) { return __uint_as_float(d & 0xffff0000u); }
DI float rbf(float f) { return bflo(cvtpk(f, 0.f)); }
DI float dot2(u32 a, u32 b, float c) { return __builtin_amdgcn_fdot2_f32_bf16(__builtin_bit_cast(bf16x2, a), __builtin_bit_cast(bf16x2, b), c, false); }
DI s16x8 cat(s16x4 lo, s16x4 hi) { return __builtin_shufflevector(lo, hi, 0, 1, 2, 3, 4, 5, 6, 7); }
DI f32x4 mfma(s16x8 a, u32x4 b, f32x4 c) { return __builtin_amdgcn_mfma_f32_16x16x32_bf16(__builtin_bit_cast(bf16x8, a), __builtin_bit_cast(bf16x8, b), c, 0, 0, 0); }
DI void mfma_v(f32x4& c, s16x8 a, u32x4 b) { asm volatile("v_mfma_f32_16x16x32_bf16 %0, %1, %2, %0" : "+v"(c) : "v"(a), "v"(b)); }
DI void mfma_v0(f32x4& c, s16x8 a, u32x4 b) { asm volatile("v_mfma_f32_16x16x32_bf16 %0, %1, %2, 0" : "=&v"(c) : "v"(a), "v"(b)); }
DI void mfma_a(f32x4& c, s16x8 a, u32x4 b) { asm volatile("v_mfma_f32_16x16x32_bf16 %0, %1, %2, %0" : "+v"(c) : "v"(a), "a"(b)); }
DI void mfma_a0(f32x4& c, s16x8 a, u32x4 b) { asm volatile("v_mfma_f32_16x16x32_bf16 %0, %1, %2, 0" : "=&v"(c) : "v"(a), "a"(b)); }
DI void mfma_acc(f32x4& c, s16x8 a, u32x4 b) { asm volatile("v_mfma_f32_16x16x32_bf16 %0, %1, %2, %0" : "+a"(c) : "v"(a), "v"(b)); }
DI void mfma_acc_a(f32x4& c, s16x8 a, u32x4 b) { asm volatile("v_mfma_f32_16x16x32_bf16 %0, %1, %2, %0" : "+a"(c) : "a"(a), "v"(b)); }
DI void pin_a(f32x4& a) { asm volatile("" : "+a"(a)); }
DI void mfma_av(f32x4& c, s16x8 a, u32x4 b) { asm volatile("v_mfma_f32_16x16x32_bf16 %0, %1, %2, %0" : "+v"(c) : "a"(a), "v"(b)); }
DI void mfma_av0(f32x4& c, s16x8 a, u32x4 b) { asm volatile("v_mfma_f32_16x16x32_bf16 %0, %1, %2, 0" : "=&v"(c) : "a"(a), "v"(b)); }
DI void mfma_aa(f32x4& c, s16x8 a, u32x4 b) { asm volatile("v_mfma_f32_16x16x32_bf16 %0, %1, %2, %0" : "+v"(c) : "a"(a), "a"(b)); }
DI void mfma_aa0(f32x4& c, s16x8 a, u32x4 b) { asm volatile("v_mfma_f32_16x16x32_bf16 %0, %1, %2, 0" : "=&v"(c) : "a"(a), "a"(b)); }
template <int OFF> DI void trrd_a(s16x4& d, u32 a) { asm volatile("ds_read_b64_tr_b16 %0, %1 offset:%2" : "=a"(d) : "v"(a), "i"(OFF) : "memory"); }
template <int OFF> DI void trrd_v(s16x4& d, u32 a) { asm volatile("ds_read_b64_tr_b16 %0, %1 offset:%2" : "=v"(d) : "v"(a), "i"(OFF) : "memory"); }
template <int OFF> DI void rd128_v(u32x4& d, u32 a) { asm volatile("ds_read_b128 %0, %1 offset:%2" : "=v"(d) : "v"(a), "i"(OFF) : "memory"); }
template <int OFF> DI void ld128_a(u32x4& d, u32 voff, srd_t srd, u32 soff) {
  asm volatile("buffer_load_dwordx4 %0, %1, %2, %3 offen offset:%4 " PAUX : "=a"(d) : "v"(voff), "s"(srd), "s"(soff), "i"(OFF) : "memory"); }
template <int N> DI void wl() { asm volatile("s_waitcnt lgkmcnt(%0)" :: "i"(N) : "memory"); }
DI void zero_v(f32x4& a) { f32x2 lo, hi; asm volatile("v_mov_b64 %0, 0\n\tv_mov_b64 %1, 0" : "=v"(lo), "=v"(hi)); a = __builtin_shufflevector(lo, hi, 0, 1, 2, 3); }
DI float fmax2(float a, float b) { return __builtin_elementwise_maximum(a, b); }
template <int R> struct FR;
#define DEF_FR(R, A0, A1, A2, A3) template <> struct FR<R> { \
  template <int OFF> static DI void lo(s16x4& d, u32 a) { asm volatile("ds_read_b64_tr_b16 %0, %1 offset:%2" : "={a[" #A0 ":" #A1 "]}"(d) : "v"(a), "i"(OFF) : "memory"); } \
  template <int OFF> static DI void hi(s16x4& d, u32 a) { asm volatile("ds_read_b64_tr_b16 %0, %1 offset:%2" : "={a[" #A2 ":" #A3 "]}"(d) : "v"(a), "i"(OFF) : "memory"); } \
  static DI void vv(f32x4& c, s16x4 l, s16x4 h, u32x4 b) { asm volatile("v_mfma_f32_16x16x32_bf16 %0, a[" #A0 ":" #A3 "], %3, %0" : "+v"(c) : "{a[" #A0 ":" #A1 "]}"(l), "{a[" #A2 ":" #A3 "]}"(h), "v"(b)); } \
  static DI void vv0(f32x4& c, s16x4 l, s16x4 h, u32x4 b) { asm volatile("v_mfma_f32_16x16x32_bf16 %0, a[" #A0 ":" #A3 "], %3, 0" : "=&v"(c) : "{a[" #A0 ":" #A1 "]}"(l), "{a[" #A2 ":" #A3 "]}"(h), "v"(b)); } \
  static DI void va(f32x4& c, s16x4 l, s16x4 h, u32x4 b) { asm volatile("v_mfma_f32_16x16x32_bf16 %0, a[" #A0 ":" #A3 "], %3, %0" : "+v"(c) : "{a[" #A0 ":" #A1 "]}"(l), "{a[" #A2 ":" #A3 "]}"(h), "a"(b)); } \
  static DI void va0(f32x4& c, s16x4 l, s16x4 h, u32x4 b) { asm volatile("v_mfma_f32_16x16x32_bf16 %0, a[" #A0 ":" #A3 "], %3, 0" : "=&v"(c) : "{a[" #A0 ":" #A1 "]}"(l), "{a[" #A2 ":" #A3 "]}"(h), "a"(b)); } \
  static DI void k16(f32x4& c, s16x4 l, u32x2 b) { asm volatile("v_mfma_f32_16x16x16_bf16 %0, a[" #A0 ":" #A1 "], %2, %0" : "+v"(c) : "{a[" #A0 ":" #A1 "]}"(l), "v"(b)); } \
  static DI void av(f32x4& c, s16x4 l, s16x4 h, u32x4 b) { asm volatile("v_mfma_f32_16x16x32_bf16 %0, a[" #A0 ":" #A3 "], %3, %0" : "+a"(c) : "{a[" #A0 ":" #A1 "]}"(l), "{a[" #A2 ":" #A3 "]}"(h), "v"(b)); } };
DEF_FR(0, 208, 209, 210, 211)
DEF_FR(1, 212, 213, 214, 215)
DEF_FR(2, 216, 217, 218, 219)
DEF_FR(3, 220, 221, 222, 223)
DEF_FR(4, 224, 225, 226, 227)
DEF_FR(5, 228, 229, 230, 231)
DEF_FR(6, 232, 233, 234, 235)
DEF_FR(7, 236, 237, 238, 239)
DEF_FR(8, 240, 241, 242, 243)
DEF_FR(9, 244, 245, 246, 247)
DEF_FR(10, 248, 249, 250, 251)
DEF_FR(11, 252, 253, 254, 255)
DI void mfma_vn(f32x4& c, s16x8 a, u32x4 b) { asm volatile("s_nop 1\n\tv_mfma_f32_16x16x32_bf16 %0, %1, %2, %0" : "+v"(c) : "v"(a), "v"(b)); }
DI void mfma_v0n(f32x4& c, s16x8 a, u32x4 b) { asm volatile("s_nop 1\n\tv_mfma_f32_16x16x32_bf16 %0, %1, %2, 0" : "=&v"(c) : "v"(a), "v"(b)); }
DI void mfma16_c(f32x4& d, u32x2 a, u32x2 b, f32x4 c) { asm volatile("v_mfma_f32_16x16x16_bf16 %0, %1, %2, %3" : "=&v"(d) : "v"(a), "v"(b), "v"(c)); }
DI void mfma16_v(f32x4& d, u32x2 a, u32x2 b) { asm volatile("v_mfma_f32_16x16x16_bf16 %0, %1, %2, %0" : "+v"(d) : "v"(a), "v"(b)); }
template <int CTRL> DI float dppadd(float v) { return v + __int_as_float(__builtin_amdgcn_update_dpp(0, __float_as_int(v), CTRL, 0xf, 0xf, true)); }
DI u32 v_lshl16(u32 a) { u32 r; asm volatile("v_lshlrev_b32 %0, 16, %1" : "=v"(r) : "v"(a)); return r; }
DI u32 v_andhi(u32 a) { u32 r; asm volatile("v_and_b32 %0, 0xffff0000, %1" : "=v"(r) : "v"(a)); return r; }
DI u32 v_fmav(u32 a, float b, float c) { u32 r; asm volatile("v_fma_f32 %0, %1, %2, %3" : "=v"(r) : "v"(a), "v"(b), "v"(c)); return r; }
DI u32 v_cvtv(u32 a, u32 b) { u32 r; asm volatile("v_cvt_pk_bf16_f32 %0, %1, %2" : "=v"(r) : "v"(a), "v"(b)); return r; }
DI u32 v_cvtf(float a, float b) { u32 r; asm volatile("v_cvt_pk_bf16_f32 %0, %1, %2" : "=v"(r) : "v"(a), "v"(b)); return r; }
DI u32 v_addv(u32 a, u32 s) { u32 r; asm volatile("v_add_u32 %0, %1, %2" : "=v"(r) : "s"(s), "v"(a)); return r; }
DI void v_swap(u32& a, u32& b) { asm volatile("v_swap_b32 %0, %1" : "+v"(a), "+v"(b)); }
DI void keep4(u32x4 a, u32x4 b, u32x4 c, u32x4 d) { asm volatile("" :: "v"(a), "v"(b), "v"(c), "v"(d)); }
DI void pin(f32x4& a) { asm volatile("" : "+v"(a)); }
DI void pin2(float& a, float& b) { asm volatile("" : "+v"(a), "+v"(b)); }
DI void zero_a(f32x4& a) { a = f32x4{0, 0, 0, 0}; asm volatile("" : "+a"(a)); }
DI u32 sopaque(u32 v) { asm volatile("" : "+s"(v)); return v; }
DI float max2(float a, float b) { float r; asm("v_max_f32 %0, %1, %2" : "=v"(r) : "v"(a), "v"(b)); return r; }
DI float max3(float a, float b, float c) { float r; asm("v_max3_f32 %0, %1, %2, %3" : "=v"(r) : "v"(a), "v"(b), "v"(c)); return r; }
DI void pin1(float& a) { asm volatile("" : "+v"(a)); }
DI void pinu(u32x4& a) { asm volatile("" : "+v"(a)); }
DI void pinu2(u32x2& a, u32x2& b) { asm volatile("" : "+v"(a), "+v"(b)); }
DI void undef_v(f32x4& a) { asm volatile("" : "=v"(a)); }
DI void undef_a(u32x4& a) { asm volatile("" : "=a"(a)); }
DI void pinw(u32& a) { asm volatile("" : "+v"(a)); }
DI void wr64_a(u32 a, u32x4 d) {
  asm volatile("ds_write_b64 %0, %1\n\tds_write_b64 %0, %2 offset:%3" :: "v"(a), "a"(__builtin_shufflevector(d, d, 0, 1)), "a"(__builtin_shufflevector(d, d, 2, 3)), "i"(80 * VROW) : "memory"); }
DI void mfma2_v(f32x4& c, s16x4 l, s16x4 h, u32x4 b) { mfma_v(c, cat(l, h), b); }
DI void mfma2_v0(f32x4& c, s16x4 l, s16x4 h, u32x4 b) { mfma_v0(c, cat(l, h), b); }
DI void ld128_vs(u32x4& d, u32 voff, srd_t srd, u32 soff) { asm volatile("buffer_load_dwordx4 %0, %1, %2, %3 offen sc1" : "=v"(d) : "v"(voff), "s"(srd), "s"(soff) : "memory"); }
DI void ld64_v(u32x2& d, u32 voff, srd_t srd, u32 soff) { asm volatile("buffer_load_dwordx2 %0, %1, %2, %3 offen" : "=v"(d) : "v"(voff), "s"(srd), "s"(soff) : "memory"); }
DI void pinw_a(u32& a) { asm volatile("" : "+a"(a)); }
DI u32x2 rd64(u32 a) { return *(LDS3 u32x2*)(size_t)a; }
DI void pins(s16x8& a) { asm volatile("" : "+v"(a)); }
DI void nop8(f32x4 (&a)[4][2]) { asm volatile("s_nop 7" : "+v"(a[0][0]), "+v"(a[0][1]), "+v"(a[1][0]), "+v"(a[1][1]), "+v"(a[2][0]), "+v"(a[2][1]), "+v"(a[3][0]), "+v"(a[3][1])); }
DI srd_t code_srd(int& last) {
  unsigned long long pc; int len;
  asm volatile("s_getpc_b64 %0\n1:\n\ts_mov_b32 %1, fused_code_end-1b" : "=s"(pc), "=s"(len));
  last = len > 2048 ? len - 2048 : 0;
  return mksrd((const void*)pc, len > 2048 ? (u32)len : 0u);
}
DI u32 rfl(u32 v) { return __builtin_amdgcn_readfirstlane(v); }
DI void st128(u32x4 d, srd_t s, u32 v, u32 so) { if (ABL & 4) v = OOB; if (ABL & 8) { __builtin_amdgcn_raw_buffer_store_b128(d, s, v, so, 0); return; } __builtin_amdgcn_raw_buffer_store_b128(d, s, v, so, 16); }
DI u32x4 ld128(srd_t s, u32 v, u32 so) { return __builtin_amdgcn_raw_buffer_load_b128(s, v, so, 16); }
DI void st_flag(u32 val, srd_t s, u32 v0, u32 so) { __builtin_amdgcn_raw_buffer_store_b32(val, s, v0, so, 16); }
DI bool flags_ok(u32 word, u32 tag, u32 need) { const u32 d = word - tag; return __all(need == 0 || (d < 16u && d >= need)); }
DI void spin(u32 voff, srd_t s, u32 so, u32 tag, u32 need) {
#pragma unroll 1
  for (u32 i = 0; i < SPIN_MAX; ++i) { u32 wd; poll_issue(wd, voff, s, so); waitvm_v<0>(wd); if (flags_ok(wd, tag, need)) break; __builtin_amdgcn_s_sleep(1); }
}
DI void spin2(u32 voff, srd_t s, u32 so, u32 lo, u32 lim) {
#pragma unroll 1
  for (u32 i = 0; i < SPIN_MAX; ++i) { __builtin_amdgcn_s_sleep(1); u32 wd; poll_issue(wd, voff, s, so); waitvm_v<0>(wd); if (__all(wd - lo <= lim)) break; }
}
DI void spin2n(u32 voff, srd_t s, u32 so, u32 lo, u32 lim) {
#pragma unroll 1
  for (u32 i = 0; i < SPIN_MAX; ++i) { u32 wd; poll_issue(wd, voff, s, so); waitvm_v<0>(wd); if (__all(wd - lo <= lim)) break; __builtin_amdgcn_s_sleep(1); }
}
#define STAMP(i)
#define STAMP_DECL
#define STAMP_FLUSH()

enum { OP_W = 1, OP_HS, OP_HB, OP_HC, OP_FS, OP_FLAG, OP_POLL, OP_MISC, OP_SL, OP_PL };
constexpr int dpos(int m) { return m == 3 ? 0 : m == 9 ? 1 : m == 15 ? 2 : m == 21 ? 3 : m == 27 ? 4 : m == 30 ? 5 : -1; }
constexpr int fpos(int m) { return (m == 6 || m == 12 || m == 18 || m == 24) ? m / 6 - 1 : (m >= 35 && m % 4 == 3) ? 4 + (m - 35) / 4 : -1; }
constexpr int f2op(int J, int m) {
  if (J <= 14 && dpos(m) >= 0) return OP_W;
  if (J == -2) return (m == 34 || m == 37 || m == 40 || m == 43 || m == 46) ? OP_MISC : 0;
  if (J == -1) return m == 46 ? OP_MISC : 0;
  const int p = fpos(m);
  if (J <= 5 && (p == 0 || p == 2 || p == 5)) return OP_HS;
  if ((J == 2 && m == 47) || (J == 6 && m == 47) || ((J == 14 || J == 16) && m == 41)) return OP_FLAG;
  if ((J == 3 && m == 43) || (J == 7 && m == 47) || J == 13 && m == 1 || J == 15 && m == 21 || J == 17 && m == 9) return OP_POLL;
  if ((J == 4 && (p == 4 || p == 6 || p == 7) ) || (J == 4 && m == 33) || (J == 5 && (p == 1 || p == 3 || p == 4 || p == 6 || p == 7 || m == 1 || m == 33 || m == 45))) return OP_HB;
  if ((J == 6 && p >= 0 && p < 7) || (J == 7 && p >= 0 && p < 5)) return OP_HC;
  if ((J == 8 && p >= 4) || (J == 9 && p >= 0)) return OP_HB;
  if ((J == 10 && p >= 0) || (J == 11 && p >= 0 && p < 4)) return OP_HC;
  if ((J == 14 || J == 16) && (m == 2 || m == 5 || m == 8 || m == 11)) return OP_FS;
  if ((J == 13 && m >= 33 && m % 2 == 1) || (J == 14 && ((m >= 12 && m < 27 && m % 3 != 0) && m != 13 && m != 25))) return OP_SL;
  if ((J == 15 && m >= 40) || (J == 17 && m >= 28 && m < 36)) return OP_PL;
  if (J == 16 && m >= 16 && m < 32 && m % 4 == 0) return OP_MISC;
  return 0;
}
constexpr int f2count(int J0, int m0, int J1, int m1) {
  int n = 0;
  for (int J = J0; J <= J1; ++J) for (int m = 0; m < 48; ++m) { if (J == J0 && m <= m0) continue; if (J == J1 && m >= m1) continue; n += f2op(J, m) != 0; }
  if (J0 < 0 && J1 >= 0) n += 6;
  return n > 63 ? 63 : n;
}
constexpr int f2index(int J, int m) {
  int n = 0; const int k = f2op(J, m);
  for (int j = 0; j <= J; ++j) for (int q = 0; q < 48; ++q) { if (j == J && q >= m) break; n += f2op(j, q) == k; }
  return n;
}
constexpr int f2find(int k, int idx) {
  int n = 0;
  for (int j = 0; j < 18; ++j) for (int q = 0; q < 48; ++q) if (f2op(j, q) == k) { if (n == idx) return 48 * j + q; ++n; }
  return -1;
}
constexpr int f2after(int k, int idx, int J, int m) { return f2count(f2find(k, idx) / 48, f2find(k, idx) % 48, J, m); }
constexpr int imin(int a, int b) { return a < b ? a : b; }
static_assert(f2find(OP_HS, 17) >= 0 && f2find(OP_HS, 18) < 0 && f2find(OP_SL, 15) >= 0 && f2find(OP_SL, 16) < 0 && f2find(OP_MISC, 3) >= 0 && f2find(OP_MISC, 4) < 0 && f2find(OP_HB, 23) >= 0 && f2find(OP_HB, 24) < 0 && f2find(OP_HC, 23) >= 0 && f2find(OP_HC, 24) < 0, "schedule");

struct XState { u32 vx[4]; srd_t srd; float rs[2], sh[2]; u32 bx; };
DI void x_dma(const XState& X, u32 ldsdst, u32 soff) {
  setm0(ldsdst);
  sfor<4>([&](auto P) { dma16<P.value * 1024>(X.vx[P.value], X.srd, soff); });
}
DI u32x4 norm_frag(u32x4 r, float rs, float sh) {
  u32x4 o;
#pragma unroll
  for (int i = 0; i < 4; ++i) o[i] = cvtpk(__builtin_fmaf(bflo(r[i]), rs, sh), __builtin_fmaf(bfhi(r[i]), rs, sh));
  return o;
}
template <int K> DI void norm_op(const XState& X, const u32x4 (&raw)[2], u32x4 (&Bn)[2], u32 (&tlo)[2], u32 (&thi)[2]) {
  constexpr int blk = K / 10, r = K % 10, i = 2 * blk + (r & 1), tb = i >> 2, d = i & 3, op = r >> 1, z = r & 1;
  if constexpr (op == 0) tlo[z] = v_lshl16(raw[tb][d]);
  if constexpr (op == 1) thi[z] = v_andhi(raw[tb][d]);
  if constexpr (op == 2) tlo[z] = v_fmav(tlo[z], X.rs[tb], X.sh[tb]);
  if constexpr (op == 3) thi[z] = v_fmav(thi[z], X.rs[tb], X.sh[tb]);
  if constexpr (op == 4) Bn[tb][d] = v_cvtv(tlo[z], thi[z]);
}
template <int N> DI void ln_group(u32x4 (&p)[9], u32 dst, u32 xw) {
  wait9<N>(p);
  wr128(xw, p[0]); wr128(xw + 4096, p[1]);
  float a0 = 0, a1 = 0, b0 = 0, b1 = 0;
#pragma unroll
  for (int c = 0; c < 9; ++c) { a0 = dot2(p[c][0], 0x3f803f80u, a0); a1 = dot2(p[c][1], 0x3f803f80u, a1); a0 = dot2(p[c][2], 0x3f803f80u, a0); a1 = dot2(p[c][3], 0x3f803f80u, a1);
    b0 = dot2(p[c][0], p[c][0], b0); b1 = dot2(p[c][1], p[c][1], b1); b0 = dot2(p[c][2], p[c][2], b0); b1 = dot2(p[c][3], p[c][3], b1); }
  const float a = dppadd<0x141>(dppadd<0x4E>(dppadd<0xB1>(a0 + a1))), b = dppadd<0x141>(dppadd<0x4E>(dppadd<0xB1>(b0 + b1)));
  const float m = a * (1.f / 576.f), var = fmaxf(b * (1.f / 576.f) - m * m, 0.f);
  const float rs = __builtin_amdgcn_rcpf(rbf(__builtin_amdgcn_sqrtf(rbf(rbf(var) + 1e-5f))));
  wr64(dst, u32x2{__float_as_uint(rs), __float_as_uint(-rbf(m) * rs)});
  asm volatile("" ::: "memory");
}
DI void epoch_issue(u32& e, const char* p) { asm volatile("s_load_dword %0, %1, 0x0" : "=s"(e) : "s"(p) : "memory"); }
DI void epoch_wait(u32& e0, u32& e1) { asm volatile("s_waitcnt lgkmcnt(0)" : "+s"(e0), "+s"(e1) :: "memory"); }

__global__ __launch_bounds__(256, 1) __attribute__((flatten)) void fused(const u16* __restrict__ x, const u16* __restrict__ wqkv, const u16* __restrict__ wo, const u16* __restrict__ w1,
                                               const u16* __restrict__ w2, u16* __restrict__ out, char* __restrict__ ws) {
  extern __shared__ __attribute__((aligned(16))) char smem[];
  STAMP_DECL;
  STAMP(0);
  const u32 ldsb = (u32)(size_t)(LDS3 char*)smem;
  const u32 l = threadIdx.x & 63, w = rfl(threadIdx.x >> 6);
  const u32 bid = blockIdx.x, xcd = bid & 7, jj = bid >> 3;
  const u32 t7 = jj & 7, tile = (xcd & 1) * 8 + t7, sq = xcd >> 1, role = jj >> 3;
  const u32 tok0 = tile * 128;
  const u32 r8 = l >> 3, s8 = l & 7;
  const u32 g = l >> 4, tk = l & 15, q4 = (l >> 2) & 3, p4 = l & 3, rr = 4 * (g & 1) + q4, hh = g >> 1;
  XState X;
  X.srd = mksrd_b(x, SEQ * XROW);
  const u32 pa0 = BIAS + (tok0 + 32 * w + 2 * r8) * XROW + 16 * s8;
  auto grp_so = [&](int i) { const u32 p = (i + XROT * role) & 3; return (16 * (p & 1) + (p >> 1)) * XROW; };
  auto ld_part = [&](u32x4 (&p)[9], int i, auto LO, auto N) { const u32 so = grp_so(i); sfor<N.value>([&](auto C) { constexpr int c = LO.value + C.value; ld128_asm<c * 128>(p[c], pa0, X.srd, so); }); };
  auto ld_group = [&](u32x4 (&p)[9], int i) { ld_part(p, i, IC(0), IC(9)); };
  auto xwr = [&](int p) { return ldsb + XBASE + w * 8192 + (8 * p + r8) * 128 + 16 * (s8 ^ (r8 >> 1) ^ (4 * (p & 1))); };
  auto sdst = [&](int i) { return ldsb + SBASE + w * 256 + (8 * ((i + XROT * role) & 3) + r8) * 8; };
  u32 ep0, ep1;
  srd_t srdWS;
  u32 v16, v0, xdst, colswz;
  auto common = [&]() {
    epoch_issue(ep0, ws + WS_EPOCH + tile * 128);
    epoch_issue(ep1, ws + WS_EPOCH + ((tile + 1) & 15) * 128);
    srdWS = mksrd(ws, WS_END);
    v16 = l * 16; v0 = l == 0 ? 0u : OOB;
#pragma unroll
    for (int p = 0; p < 4; ++p) X.vx[p] = BIAS + (tok0 + 32 * w + 16 * (p & 1) + 2 * r8 + (p >> 1)) * XROW + 16 * (s8 ^ (r8 >> 1) ^ (4 * (p & 1))) - 1024 * p;
#pragma unroll
    for (int p = 0; p < 4; ++p) pinw(X.vx[p]);
    X.bx = ldsb + XBASE + w * 8192 + tk * 128 + 16 * (g ^ (tk >> 1));
    xdst = ldsb + XBASE + w * 8192;
    colswz = 32 * ((s8 >> 1) ^ (r8 >> 1)) + 16 * (s8 & 1);
  };
  auto ln_fetch = [&]() {
    u32x2 t0, t1;
    asm volatile("ds_read_b64 %0, %2\n\tds_read_b64 %1, %2 offset:128\n\ts_waitcnt lgkmcnt(0)" : "=&v"(t0), "=&v"(t1) : "v"(ldsb + SBASE + w * 256 + tk * 8) : "memory");
    X.rs[0] = __uint_as_float(t0[0]); X.sh[0] = __uint_as_float(t0[1]); X.rs[1] = __uint_as_float(t1[0]); X.sh[1] = __uint_as_float(t1[1]); };
  if (__builtin_expect(role < 3, 1)) {
    u32x4 pA[9], pB[9];
    ld_group(pA, 0); ld_part(pB, 1, IC(0), IC(NFIRST - 9));
    SB0();
    common();
    const u32 slice = sq * 3 + role;
    const u32 om = (sq + role) & 3;
    const u32 gm[3] = {om == 0 ? 1u : om == 1 ? 2u : 0u, om == 0 ? 2u : om == 1 ? 0u : 1u, om == 3 ? 2u : om};
    srd_t dsrd = mksrd_b(w1, DM * W1ROW);
    u32 dvl = (8 * (r8 >> 2) + (r8 & 3)) * W1ROW + colswz;
    u32 dk[6];
#pragma unroll
    for (int c = 0; c < 6; ++c) { const u32 pi = 6 * w + c; dk[c] = BIAS + (16 * (pi & 1) + 4 * ((pi >> 1) & 1)) * W1ROW + slice * 768 + (pi >> 2) * 128 - 1024 * (c & 3); }
    u32 dso = 0, dd1 = 32 * W1ROW, dd2 = 32 * W1ROW, dwrap = ~0u;
    const u32 dm0lo = ldsb + w * 6144, dm0hi = dm0lo + FNS * FSLOT;
    u32 dm0 = dm0lo;
    auto dma_piece = [&](auto C) { constexpr int c = C.value; dma16<(c & 3) * 1024>(dvl, dsrd, dso + dk[c]); };
    auto dma_adv = [&](auto E) {
      if constexpr (E.value == 0) dso += dd1; else { dso += dd2; dso = dso == dwrap ? 0 : dso; }
      dm0 += FSLOT; dm0 = dm0 == dm0hi ? dm0lo : dm0; };
    auto dma_all = [&](auto E) { setm0(dm0); sfor<4>(dma_piece); setm0(dm0 + 4096); dma_piece(IC(4)); dma_piece(IC(5)); dma_adv(E); };
    constexpr int NVA = 20;
    f32x4 acc[24][2];
    auto zacc = [&](auto LO, auto N) { sfor<N.value>([&](auto I) { constexpr int k = LO.value + I.value; if constexpr (k < 2 * NVA) zero_v(acc[k >> 1][k & 1]); else zero_a(acc[k >> 1][k & 1]); }); };
    zacc(IC(0), IC(48));
    u32 cur[4], nxt[4];
    u32 rdelta = FSLOT, rslot = 0;
    constexpr int LA = 8, NF = 12;
    s16x4 Fl[NF], Fh[NF];
    auto rdhalf = [&](auto A, auto T, const u32 (&bs)[4]) { constexpr int a = A.value;
      if constexpr (T.value == 0) FR<a % NF>::template lo<(a >> 2) * 4096>(Fl[a % NF], bs[a & 3]); else FR<a % NF>::template hi<(a >> 2) * 4096 + 2048>(Fh[a % NF], bs[a & 3]); };
#pragma unroll
    for (int u = 0; u < 4; ++u) cur[u] = ldsb + hh * 1024 + 128 * rr + 32 * (u ^ (rr >> 1)) + 8 * p4;
    srd_t psrd = mksrd(w2, DFF * XROW);
    u32 pso_ = (slice * 384 + 48 * t7) * XROW + w * 1024;
    u32 plim_ = (slice * 384 + 48 * t7 + 48) * XROW - 1024;
    ld_part(pB, 1, IC(NFIRST - 9), IC(18 - NFIRST));
    dma_all(IC(0));
    ln_group<15>(pA, sdst(0), xwr(0));
    ld_group(pA, 2);
    ln_group<15>(pB, sdst(1), xwr(1));
    ld_group(pB, 3);
    ln_group<9>(pA, sdst(2), xwr(2));
    dma_all(IC(1));
    __builtin_amdgcn_s_barrier();
    u32x4 B[2], Bn[2], raw[2];
    sfor<LA>([&](auto A) { rdhalf(A, IC(0), cur); rdhalf(A, IC(1), cur); });
    ln_group<6>(pB, sdst(3), xwr(3));
    rd128_v<0>(raw[0], X.bx); rd128_v<2048>(raw[1], X.bx);
    setm0(ldsb + TBASE); dma16<0>(v16, psrd, pso_);
    dma_all(IC(0));
    setm0(ldsb + TBASE); dma16<0>(v16, psrd, pso_);
    STAMP(1);
    ln_fetch();
    epoch_wait(ep0, ep1);
    pinu(raw[0]); pinu(raw[1]);
    B[0] = norm_frag(raw[0], X.rs[0], X.sh[0]); B[1] = norm_frag(raw[1], X.rs[1], X.sh[1]);
    asm volatile("s_nop 1" : "+v"(B[0]), "+v"(B[1]));
    STAMP(2);
    auto stage = [&](auto E, auto NV, auto DMA, auto L8, auto&& MM, auto&& MEM, auto&& EX, auto&& BAR) {
      sfor<48>([&](auto M) { constexpr int m = M.value, a = m >> 1, tb = m & 1;
        if constexpr (m == 32 && NV.value >= 0) { if constexpr (NV.value < 63) waitvm<NV.value>(); __builtin_amdgcn_s_barrier(); BAR(); }
        if constexpr (m % 8 == 0) wl<(NV.value < 0 && m >= 40) ? 0 : (m == 8 ? L8.value : 8)>();
        MM(M);
        if constexpr (a + LA < 24) rdhalf(IC(a + LA), IC(tb), cur); else if constexpr (NV.value >= 0) rdhalf(IC(a + LA - 24), IC(tb), nxt);
        if constexpr (DMA.value && dpos(m) >= 0) dma_piece(IC(dpos(m)));
        MEM(M);
        SB0();
        if constexpr (DMA.value && m == 2) setm0_add<0>(dm0);
        if constexpr (DMA.value && m == 26) setm0_add<4096>(dm0);
        if constexpr (DMA.value && m == 31) dma_adv(IC(E.value ^ 1));
        if constexpr (m == 1) { rslot += FSLOT; rdelta = rslot == FNS * FSLOT ? 0u - (FNS - 1) * FSLOT : FSLOT; rslot = rslot == FNS * FSLOT ? 0 : rslot; }
        if constexpr (m >= 2 && m < 6 && NV.value >= 0) nxt[m - 2] = v_addv(cur[m - 2], rdelta);
        EX(M);
        SB0();
      });
#pragma unroll
      for (int u = 0; u < 4; ++u) cur[u] = nxt[u];
    };
    auto nop = [&](auto) {};
    auto nop0 = [&]() {};
    constexpr auto tbo = [](int m) { return GRAY ? ((m ^ (m >> 1)) & 1) : (m & 1); };
    u32 xs = 0, xso = 256, ba = X.bx ^ 64;
    u32 tlo[2], thi[2];
    for (int it = 0; it < 9; ++it) {
      sfor<2>([&](auto E) { constexpr int e = E.value;
        if constexpr (e == 0) { if (it == 7) { X.srd = mksrd(x, 0); if (!(ABL & 16)) { int last; psrd = code_srd(last); plim_ = (u32)last; pso_ = (t7 & 1) * 16384 + w * 1024; pso_ = pso_ > plim_ ? plim_ : pso_; } } }
        if constexpr (e == 1) { if (it == 7) {
            dsrd = mksrd_b(w2, DFF * XROW); dvl = r8 * XROW + colswz;
#pragma unroll
            for (int c = 0; c < 6; ++c) dk[c] = BIAS + (48 * w + 8 * c) * XROW + role * 384 - 1024 * (c & 3);
            dso = slice * 384 * XROW + gm[0] * 128; dd1 = 192 * XROW; dd2 = (gm[1] - gm[0]) * 128 - 192 * XROW;
          } }
        stage(E, IC(e == 0 ? 14 : 18), IC(1), IC(6),
              [&](auto M) { constexpr int a = M.value >> 1, tb = tbo(M.value); if constexpr (a < NVA) FR<a % NF>::vv(acc[a][tb], Fl[a % NF], Fh[a % NF], B[tb]); else FR<a % NF>::av(acc[a][tb], Fl[a % NF], Fh[a % NF], B[tb]); },
              [&](auto M) { constexpr int m = M.value;
                if constexpr (m == 0) rd128_v<0>(raw[0], ba);
                if constexpr (m == 1) rd128_v<2048>(raw[1], ba);
                if constexpr (e == 0 && m >= 34 && m < 46 && (m - 34) % 3 == 0) dma16<((m - 34) / 3) * 1024>(X.vx[(m - 34) / 3], X.srd, xso);
                if constexpr (m == 46) dma16<0>(v16, psrd, pso_); },
              [&](auto M) { constexpr int m = M.value;
                if constexpr (e == 0 && m == 33) setm0_add<0>(xdst + xs);
                if constexpr (e == 0 && m == 44) { xso += 128; xs ^= 4096; }
                if constexpr (m == 45) setm0_add<TBASE>(ldsb);
                if constexpr (m == 47) { pso_ += 4096; pso_ = pso_ > plim_ ? plim_ : pso_; }
                if constexpr (m == 6) ba = v_addv(e == 0 ? X.bx : (X.bx ^ 64), e == 0 ? (xs ^ 4096) : xs);
                if constexpr (m >= 8) norm_op<m - 8>(X, raw, Bn, tlo, thi); },
              nop0);
        B[0] = Bn[0]; B[1] = Bn[1];
        asm volatile("s_nop 0" : "+v"(B[0]), "+v"(B[1]));
      });
    }
    STAMP(3);
    asm volatile("s_nop 7");
    u32x4 H[12][2];
    const u32 hb0 = WS_H + ((tile * 12 + slice) * 4 + w) * 24576;
#pragma unroll
    for (int k = 0; k < 12; ++k)
#pragma unroll
      for (int tb = 0; tb < 2; ++tb) {
        if (2 * k < NVA) { pin(acc[2 * k][tb]); pin(acc[2 * k + 1][tb]); } else { pin_a(acc[2 * k][tb]); pin_a(acc[2 * k + 1][tb]); }
#pragma unroll
        for (int i = 0; i < 4; ++i) { const f32x4 s = acc[2 * k + (i >> 1)][tb];
          H[k][tb][i] = __builtin_bit_cast(u32, __builtin_elementwise_max(__builtin_bit_cast(s16x2, cvtpk(s[2 * (i & 1)], s[2 * (i & 1) + 1])), s16x2{0, 0})); }
        if (k < 3) st128(H[k][tb], srdWS, v16 + ((2 * k + tb) & 3) * 1024, hb0 + (k >> 1) * 4096);
      }
    const u32 tag = (ep0 + 1) << 4;
    const u32 fso = WS_FLAG + (tile * 4 + w) * 128;
    const u32 slB = sq * 3 + (role == 2 ? 0 : role + 1), slC = sq * 3 + (role == 0 ? 2 : role - 1);
    const u32 sbase[3] = {slice * 384 * XROW, slB * 384 * XROW, slC * 384 * XROW};
    const u32 hbB = WS_H + ((tile * 12 + slB) * 4 + w) * 24576, hbC = WS_H + ((tile * 12 + slC) * 4 + w) * 24576;
    const u32 pg0 = WS_P + ((tile * 9 + role * 3) * 32 + w) * 4096;
    const u32 vpoll = l < 16 ? l * 4 : OOB;
    const u32 lim12 = (l == slB || l == slC) ? 13u : ~0u;
    const bool owner = om < 3;
    const u32 pown = pg0 + gm[2] * 131072;
    const u32 sqi[3] = {(om + 2 >= 3 ? om - 1 : om + 2) - role & 3, (om + 1 >= 3 ? om - 2 : om + 1) - role & 3, (3 - role) & 3};
    const u32 ned[3] = {3, 4, 3 + om};
    bool got[4] = {!owner, !owner, !owner, !owner}, pend[4] = {false, false, false, false};
    f32x4 ac3[3][4][2], acs[4][2];
    u32x4 HB[12][2], HC[12][2], sd[4], SP[4][4], PQ[3][4];
    u32 pw, pvS = OOB, pv0 = OOB, pv1 = OOB, pb1 = 0;
    s16x8 sel0, sel1;
    auto rdy = [&](auto I) { constexpr int i = I.value;
      if constexpr (i < 3) return __all(l != sqi[i] * 3 + role || pw - (tag + ned[i]) <= 15u - ned[i]);
      else return __all(l < 12 || l >= 16 || pw - (tag + role * 3 + gm[2] + 1) <= 14u - (role * 3 + gm[2])); };
    auto red_f = [&](u32x4 (&P)[4]) { sfor<4>([&](auto U) { mfma_a(ac3[2][U.value][0], sel0, P[U.value]); mfma_a(ac3[2][U.value][1], sel1, P[U.value]); }); };
    auto red_s = [&]() { sfor<16>([&](auto I) { constexpr int q = I.value >> 2, u = I.value & 3; mfma_a(acs[u][0], sel0, SP[q][u]); mfma_a(acs[u][1], sel1, SP[q][u]); }); };
    auto ld_s = [&](auto I) { constexpr int i = I.value; ld128_a<(i & 3) * 1024>(SP[i >> 2][i & 3], pvS, srdWS, pown + (4 + (i >> 2)) * 16384); };
    const srd_t srdX = mksrd_b(x, SEQ * XROW), srdO = mksrd_b(out, SEQ * XROW);
    const u32 ecol = (role * 3 + gm[2]) * 128;
    asm volatile("s_nop 1");
    STAMP(4);
    sfor<18>([&](auto JJ) { constexpr int J = JJ.value;
      constexpr auto whf = [](int j) { return j < 6 ? 0 : j < 9 ? 1 : j < 12 ? 2 : 1 + (j & 1); };
      constexpr auto gaf = [](int j) { return j < 6 ? j >> 1 : j < 12 ? j % 3 : (j - 12) >> 1; };
      constexpr auto haf = [](int j) { return j < 6 ? j & 1 : j < 12 ? 0 : 1; };
      constexpr int which = whf(J), gam = gaf(J), half = haf(J), Jn = J + 4;
      constexpr int nw = J <= 16 ? f2count(J - 2, 30, J, 32) : -1;
      constexpr int nv = J == 4 ? imin(nw, f2after(OP_POLL, 0, J, 32)) : J == 8 ? imin(nw, f2after(OP_POLL, 1, J, 32)) : J == 13 ? imin(nw, f2after(OP_POLL, 2, J, 32)) : nw;
      stage(IC(J & 1), IC(nv), IC(J <= 14), IC(8),
            [&](auto M) { constexpr int m = M.value, a = m >> 1, tb = tbo(m), u = a & 3, ks = a >> 2, kap = 6 * half + ks;
              if constexpr (m % 8 == 0 && J == 6) waitvm<f2after(OP_HB, 2 * ks + 1, J, m)>();
              if constexpr (m == 0 && J == 9) waitvm<f2after(OP_HC, 11, J, 0)>();
              if constexpr (m == 0 && J == 12) waitvm<f2after(OP_HB, 23, J, 0)>();
              if constexpr (m == 0 && J == 13) waitvm<f2after(OP_HC, 23, J, 0)>();
              if constexpr (m == 0 && J == 16) { waitvm<f2after(OP_SL, 15, J, 0)>(); if (pend[3]) red_s(); }
              if constexpr (m == 0 && J == 17) { waitvm<f2after(OP_PL, 7, J, 0)>(); if (pend[0]) red_f(PQ[0]); if (pend[2]) red_f(PQ[2]); pend[0] = pend[2] = pend[3] = false; }
              if constexpr (which == 0) { if constexpr (half == 0 && ks == 0) FR<a % NF>::vv0(ac3[gam][u][tb], Fl[a % NF], Fh[a % NF], H[0][tb]); else FR<a % NF>::vv(ac3[gam][u][tb], Fl[a % NF], Fh[a % NF], H[kap][tb]); }
              else if constexpr (which == 1) FR<a % NF>::va(ac3[gam][u][tb], Fl[a % NF], Fh[a % NF], HB[kap][tb]);
              else FR<a % NF>::va(ac3[gam][u][tb], Fl[a % NF], Fh[a % NF], HC[kap][tb]); },
            [&](auto M) { constexpr int m = M.value, op = f2op(J, m), i = f2index(J, m);
              if constexpr (op == OP_HS) st128(H[3 + (i >> 1)][i & 1], srdWS, v16 + ((i + 2) & 3) * 1024, hb0 + ((i + 6) >> 2) * 4096);
              if constexpr (op == OP_HB) ld128_a<(i & 3) * 1024>(HB[i >> 1][i & 1], v16, srdWS, hbB + (i >> 2) * 4096);
              if constexpr (op == OP_HC) ld128_a<(i & 3) * 1024>(HC[i >> 1][i & 1], v16, srdWS, hbC + (i >> 2) * 4096);
              if constexpr (op == OP_FS) st128(sd[i & 3], srdWS, v16 + (i & 3) * 1024, pg0 + gm[i >> 2] * 131072 + sq * 16384);
              if constexpr (op == OP_SL) ld_s(IC(i));
              if constexpr (op == OP_PL) { constexpr int q = (i & 4) ? 2 : (i >> 3); ld128_a<(i & 3) * 1024>(PQ[q][i & 3], (i & 4) ? pv1 : pv0, srdWS, pown + sqi[q] * 16384); }
              if constexpr (op == OP_MISC) { if constexpr (i < 4) dma16<i * 1024>(X.vx[i], srdX, ecol); }
              if constexpr (op == OP_POLL) poll_issue(pw, vpoll, srdWS, fso);
              if constexpr (op == OP_FLAG) {
                waitvm<(i == 0 ? f2after(OP_HS, 5, J, m) : i == 1 ? f2after(OP_HS, 17, J, m) : f2after(OP_FS, 4 * (i - 2) + 3, J, m))>();
                st_flag(tag + 1 + i, srdWS, v0, fso + slice * 4); }
              if constexpr ((J == 14 || J == 16) && m == 13) keep4(sd[0], sd[1], sd[2], sd[3]); },
            [&](auto M) { constexpr int m = M.value;
              if constexpr (m == 40 && Jn < 18) dso = sbase[whf(Jn)] + haf(Jn) * 192 * XROW + gm[gaf(Jn)] * 128;
              if constexpr ((J == 14 || J == 16) && m < 8) { constexpr int u = m >> 1, tb = m & 1, gp = (J - 14) >> 1;
                sd[u][2 * tb] = v_cvtf(ac3[gp][u][tb][0], ac3[gp][u][tb][1]); sd[u][2 * tb + 1] = v_cvtf(ac3[gp][u][tb][2], ac3[gp][u][tb][3]); }
              if constexpr (J == 10 && m >= 8 && m < 16) zero_v(acs[(m - 8) >> 1][m & 1]);
              if constexpr (J == 10 && m == 20) {
                const u32 on = (tk >> 2) == g ? 0x3f80u << (16 * (tk & 1)) : 0u, s0 = (tk & 2) ? 0u : on, s1 = (tk & 2) ? on : 0u;
                sel0 = __builtin_bit_cast(s16x8, u32x4{s0, s1, 0u, 0u}); sel1 = __builtin_bit_cast(s16x8, u32x4{0u, 0u, s0, s1}); pins(sel0); pins(sel1); }
              if constexpr (J == 16 && m == 15) setm0_add<0>(xdst);
              if constexpr (J == 15 && m == 38) { waitvm<f2after(OP_POLL, 3, J, 39)>(); waitvm_v<63>(pw);
                pend[0] = !got[0] && rdy(IC(0)); pend[2] = !got[2] && rdy(IC(2)); got[0] |= pend[0]; got[2] |= pend[2]; pv0 = pend[0] ? v16 : OOB; pv1 = pend[2] ? v16 : OOB; }
              if constexpr (J == 17 && m == 26) { waitvm<f2after(OP_POLL, 4, J, 27)>(); waitvm_v<63>(pw);
                pend[1] = !got[1] && rdy(IC(1)); pend[2] = !got[2] && rdy(IC(2)); got[1] |= pend[1]; got[2] |= pend[2]; pv0 = pend[1] ? v16 : OOB; pv1 = pend[2] ? v16 : OOB; } },
            [&]() {
              if constexpr (J == 4) { waitvm_v<63>(pw); if (!__all(pw - (tag + 1) <= lim12 + (lim12 != ~0u))) spin2(vpoll, srdWS, fso, tag + 1, lim12 + (lim12 != ~0u)); }
              if constexpr (J == 8) { waitvm_v<63>(pw); if (!__all(pw - (tag + 2) <= lim12)) spin2(vpoll, srdWS, fso, tag + 2, lim12); }
              if constexpr (J == 13) { waitvm_v<63>(pw); pend[3] = !got[3] && rdy(IC(3)); got[3] |= pend[3]; pvS = pend[3] ? v16 : OOB; } });
      if constexpr (J == 5) STAMP(5);
    });
    STAMP(6);
    nop8(ac3[2]);
    if (!owner) {
      sfor<8>([&](auto I) { constexpr int u = I.value >> 1, tb = I.value & 1;
        sd[u][2 * tb] = v_cvtf(ac3[2][u][tb][0], ac3[2][u][tb][1]); sd[u][2 * tb + 1] = v_cvtf(ac3[2][u][tb][2], ac3[2][u][tb][3]); });
      asm volatile("s_nop 0");
      sfor<4>([&](auto U) { st128(sd[U.value], srdWS, v16 + U.value * 1024, pown + sq * 16384); });
      waitvm<0>();
      st_flag(tag + 5, srdWS, v0, fso + slice * 4);
      STAMP(7);
      STAMP_FLUSH();
      return;
    }
#pragma unroll 1
    for (u32 it = 0; it < SPIN_MAX; ++it) {
      const bool done = got[0] && got[1] && got[2] && got[3];
      if (!done) poll_issue(pw, vpoll, srdWS, fso);
      waitvm_v<0>(pw);
      if (pend[0]) red_f(PQ[0]); if (pend[1]) red_f(PQ[1]); if (pend[2]) red_f(PQ[2]); if (pend[3]) red_s();
      if (done) break;
      sfor<3>([&](auto I) { constexpr int i = I.value; pend[i] = !got[i] && rdy(I);
        if (pend[i]) { got[i] = true; sfor<4>([&](auto U) { ld128_a<U.value * 1024>(PQ[i][U.value], v16, srdWS, pown + sqi[i] * 16384); }); } });
      pend[3] = !got[3] && rdy(IC(3));
      if (pend[3]) { got[3] = true; pvS = v16; sfor<16>(ld_s); }
    }
    asm volatile("s_nop 7");
    const u32 ia0 = xdst + tk * 128 + 16 * (hh ^ (tk >> 1)) + 8 * (g & 1);
#pragma unroll
    for (int u = 0; u < 4; ++u)
#pragma unroll
      for (int tb = 0; tb < 2; ++tb) {
        pin(ac3[2][u][tb]); pin(acs[u][tb]);
        const u32 ia = (ia0 ^ (32 * u)) + tb * 2048;
        const u32x2 xv = rd64(ia);
        u32x2 o;
#pragma unroll
        for (int d = 0; d < 2; ++d) {
          const f32x2 f = {ac3[2][u][tb][2 * d], ac3[2][u][tb][2 * d + 1]}, sv = {acs[u][tb][2 * d], acs[u][tb][2 * d + 1]};
          const f32x2 t = f * -1.4426950408889634f;
          const f32x2 e = f32x2{__builtin_amdgcn_exp2f(t[0]), __builtin_amdgcn_exp2f(t[1])} + 1.f;
          const f32x2 gt = f * f32x2{__builtin_amdgcn_rcpf(e[0]), __builtin_amdgcn_rcpf(e[1])};
          const f32x2 r = __builtin_elementwise_fma(sv, gt, f32x2{bflo(xv[d]), bfhi(xv[d])});
          o[d] = cvtpk(r[0], r[1]);
        }
        wr64(ia, o);
      }
#pragma unroll
    for (int c = 0; c < 4; ++c) __builtin_amdgcn_raw_buffer_store_b128(rd128(xdst + c * 1024 + v16), srdO, X.vx[c] + c * 1024, ecol, 0);
    if (role == 0 && om == 0 && w == 0) st_flag(ep0 + 1, srdWS, v0, WS_EPOCH + tile * 128);
    STAMP(7);
    STAMP_FLUSH();
  } else {
    u32x4 pA[9], pB[9];
    ld_group(pA, 0); ld_part(pB, 1, IC(0), IC(NFIRST - 9));
    SB0();
    common();
    srd_t dsrd = mksrd_b(wqkv, DM * WQROW);
    u32 dv[7];
#pragma unroll
    for (int c = 0; c < 7; ++c) { u32 pi = 7 * w + c; pi = pi > 26 ? 26 : pi;
      const u32 seg = pi / 9, idx = (pi - seg * 9) * 64 + l, rho = idx / 18, ch = idx - rho * 18;
      const u32 krow = 16 * ((rho >> 3) & 1) + 4 * (rho >> 4) + 8 * ((rho >> 2) & 1) + (rho & 3);
      dv[c] = BIAS + krow * WQROW + seg * 1152 + sq * 288 + ch * 16 - 1024 * (c & 3); }
    u32 dso = 0;
    const u32 dm0lo = ldsb + w * 7168, dm0hi = dm0lo + ANS * ASLOT;
    u32 dm0 = dm0lo;
    auto dma_piece = [&](auto C) { constexpr int c = C.value; dma16<(c & 3) * 1024>(dv[c], dsrd, dso); };
    auto dma_adv = [&]() { dso += 32 * WQROW; dm0 += ASLOT; dm0 = dm0 == dm0hi ? dm0lo : dm0; };
    auto dma_all = [&]() { setm0(dm0); sfor<4>(dma_piece); setm0(dm0 + 4096); dma_piece(IC(4)); dma_piece(IC(5)); dma_piece(IC(6)); dma_adv(); };
    f32x4 acc[27][2];
    auto zacc = [&](auto LO, auto N) { sfor<N.value>([&](auto I) { constexpr int k = LO.value + I.value; if constexpr (k < 18) zero_a(acc[k >> 1][k & 1]); else zero_v(acc[k >> 1][k & 1]); }); };
    zacc(IC(0), IC(54));
    u32 cur = ldsb + (8 * hh + rr) * 288 + 8 * p4, nxt = cur, rslot = 0, rdelta = ASLOT;
    constexpr int LA = 6;
    s16x4 Fl[9], Fh[9];
    auto rdhalf = [&](auto A, auto T, u32 bs) { constexpr int a = A.value, off = (a / 9) * 9216 + (a % 9) * 32;
      if constexpr (T.value == 0) FR<a % 9>::template lo<off>(Fl[a % 9], bs); else FR<a % 9>::template hi<off + 16 * 288>(Fh[a % 9], bs); };
    const srd_t psrd = mksrd(wqkv, DM * WQROW);
    const u32 pf0 = l < 48 ? (4 * t7 + l / 12) * WQROW + ((sq * 288) & ~127u) + (l % 12) * 32 : OOB;
    u32 pfl = w < 3 ? pf0 + w * 1152 : l * 32;
    srd_t psrdl = w < 3 ? psrd : mksrd(wo, DM * XROW);
    const int plo_ = w < 3 ? 0 : (144 * sq + 18 * t7) * XROW;
    int phi_ = w < 3 ? 17 * 32 * WQROW : plo_ + 18 * XROW - 2048, pst_ = w < 3 ? 32 * WQROW : 2048;
    int pso_ = w < 3 ? 6 * 32 * WQROW : plo_ - 7 * 2048;
    ld_part(pB, 1, IC(NFIRST - 9), IC(18 - NFIRST));
    dma_all();
    ln_group<16>(pA, sdst(0), xwr(0));
    ld_group(pA, 2);
    ln_group<16>(pB, sdst(1), xwr(1));
    ld_group(pB, 3);
    ln_group<9>(pA, sdst(2), xwr(2));
    dma_all();
    __builtin_amdgcn_s_barrier();
    u32x4 B[2], Bn[2], raw[2];
    sfor<LA>([&](auto A) { rdhalf(A, IC(0), cur); rdhalf(A, IC(1), cur); });
    ln_group<7>(pB, sdst(3), xwr(3));
    rd128_v<0>(raw[0], X.bx); rd128_v<2048>(raw[1], X.bx);
    setm0(ldsb + TBASE); sfor<3>([&](auto S) { touch(pf0, psrd, (2 + w) * 32 * WQROW + S.value * 1152); });
    STAMP(1);
    ln_fetch();
    epoch_wait(ep0, ep1);
    pinu(raw[0]); pinu(raw[1]);
    B[0] = norm_frag(raw[0], X.rs[0], X.sh[0]); B[1] = norm_frag(raw[1], X.rs[1], X.sh[1]);
    asm volatile("s_nop 1" : "+v"(B[0]), "+v"(B[1]));
    STAMP(2);
    u32 xs = 0, xso = 256, ba = X.bx ^ 64;
    u32 tlo[2], thi[2];
    for (int it = 0; it < 9; ++it) {
      if (it == 6 && w < 3 && !(ABL & 16)) { psrdl = code_srd(phi_); pfl = l * 32; pso_ = (int)w * 2048 > phi_ ? phi_ : (int)w * 2048; pst_ = 6144; }
      if (it == 7) X.srd = mksrd(x, 0);
      if (it == 8) dsrd = mksrd(wqkv, 0);
      sfor<2>([&](auto E) { constexpr int e = E.value;
        sfor<54>([&](auto M) { constexpr int m = M.value, a = m >> 1, tb = m & 1, tq = GRAY ? ((m ^ (m >> 1)) & 1) : tb;
          if constexpr (m == 42) { waitvm<e == 0 ? 8 : 12>(); __builtin_amdgcn_s_barrier(); }
          if constexpr (m % 6 == 0) wl<6>();
          if constexpr (a < 9) FR<a % 9>::av(acc[a][tq], Fl[a % 9], Fh[a % 9], B[tq]); else FR<a % 9>::vv(acc[a][tq], Fl[a % 9], Fh[a % 9], B[tq]);
          if constexpr (a + LA < 27) rdhalf(IC(a + LA), IC(tb), cur); else rdhalf(IC(a + LA - 27), IC(tb), nxt);
          if constexpr (m % 6 == 3 && m < 42) dma_piece(IC(m / 6));
          if constexpr (m == 0) rd128_v<0>(raw[0], ba);
          if constexpr (m == 1) rd128_v<2048>(raw[1], ba);
          if constexpr (e == 0 && m >= 44 && m < 52 && (m & 1) == 0) dma16<((m - 44) / 2) * 1024>(X.vx[(m - 44) / 2], X.srd, xso);
          if constexpr (m == 52) touch(pfl, psrdl, (u32)(pso_ < plo_ ? plo_ : pso_));
          SB0();
          if constexpr (m == 2) setm0_add<0>(dm0);
          if constexpr (m == 26) setm0_add<4096>(dm0);
          if constexpr (m == 40) dma_adv();
          if constexpr (e == 0 && m == 43) setm0_add<0>(xdst + xs);
          if constexpr (e == 0 && m == 53) { xso += 128; xs ^= 4096; }
          if constexpr (m == 51) setm0_add<TBASE>(ldsb);
          if constexpr (m == 53) { pso_ += pst_; pso_ = pso_ > phi_ ? phi_ : pso_; }
          if constexpr (m == 1) { rslot += ASLOT; rdelta = rslot == ANS * ASLOT ? 0u - (ANS - 1) * ASLOT : ASLOT; rslot = rslot == ANS * ASLOT ? 0 : rslot; }
          if constexpr (m == 4) nxt = v_addv(cur, rdelta);
          if constexpr (m == 6) ba = v_addv(e == 0 ? X.bx : (X.bx ^ 64), e == 0 ? (xs ^ 4096) : xs);
          if constexpr (m >= 12 && m < 52) norm_op<m - 12>(X, raw, Bn, tlo, thi);
          SB0();
        });
        cur = nxt; B[0] = Bn[0]; B[1] = Bn[1];
      });
    }
    STAMP(3);
    asm volatile("s_nop 7\n\ts_waitcnt vmcnt(0) lgkmcnt(0)" ::: "memory");
    u32x4 Kp[9], Kq[9], Vp[9];
    u32x2 Qp[9][2];
#pragma unroll
    for (int b = 0; b < 9; ++b) {
      pin(acc[9 + b][0]); pin(acc[9 + b][1]); pin(acc[18 + b][0]); pin(acc[18 + b][1]);
      Kp[b] = u32x4{cvtpk(acc[9 + b][0][0], acc[9 + b][0][1]), cvtpk(acc[9 + b][0][2], acc[9 + b][0][3]), cvtpk(acc[9 + b][1][0], acc[9 + b][1][1]), cvtpk(acc[9 + b][1][2], acc[9 + b][1][3])};
      Vp[b] = u32x4{cvtpk(acc[18 + b][0][0], acc[18 + b][0][1]), cvtpk(acc[18 + b][0][2], acc[18 + b][0][3]), cvtpk(acc[18 + b][1][0], acc[18 + b][1][1]), cvtpk(acc[18 + b][1][2], acc[18 + b][1][3])};
    }
    if (w == 3 && tile < 15) {
      const u32 hso = WS_HALO + ((tile + 1) * 4 + sq) * 18432;
#pragma unroll
      for (int b = 0; b < 9; ++b) { st128(Kp[b], srdWS, v16, hso + b * 1024); st128(Vp[b], srdWS, v16, hso + 9216 + b * 1024); }
    }
    __builtin_amdgcn_s_barrier();
    const srd_t wsrd = mksrd_b(wo, DM * XROW);
    const u32 wvl = r8 * XROW + colswz;
    constexpr auto wslot = [](int G) { return G < 3 ? 57344 + G * 20480 : G < 5 ? (G - 3) * 20480 : G == 5 ? (int)XBASE : 57344 + (G - 6) * 20480; };
    u32 wk[5];
#pragma unroll
    for (int c = 0; c < 5; ++c) { u32 pi = 5 * w + c; pi = pi > 17 ? 17 : pi; wk[c] = BIAS + (144 * sq + 8 * pi) * XROW - 1024 * (c & 3); }
    const u32 wm0 = ldsb + w * 5120;
    auto wo_piece = [&](auto GG, auto C) { constexpr int G = GG.value, c = C.value;
      if constexpr ((c & 3) == 0) { setm0_add<wslot(G) + c * 1024>(wm0); asm volatile("s_nop 0"); }
      dma16<(c & 3) * 1024>(wvl, wsrd, wk[c] + G * 128); };
    auto wo_dma = [&](auto GG) { sfor<5>([&](auto C) { wo_piece(GG, C); }); };
    const u32 vw = ldsb + (16 * (w + 1) + tk) * VROW + 8 * g;
    auto wr_v = [&]() {
#pragma unroll
      for (int b = 0; b < 9; ++b) { wr64(vw + 32 * b, u32x2{Vp[b][0], Vp[b][1]}); wr64(vw + 80 * VROW + 32 * b, u32x2{Vp[b][2], Vp[b][3]}); } };
    if (w < 3) {
      wr_v();
#pragma unroll
      for (int b = 0; b < 9; ++b) wr128(ldsb + XBASE + w * 9216 + b * 1024 + v16, Kp[b]);
      waitlgkm<0>();
    } else if (tile < 15) { waitvm<0>(); st_flag(((ep1 + 1) << 4) + 1, srdWS, v0, WS_HFLAG + ((tile + 1) * 4 + sq) * 128); }
    __builtin_amdgcn_s_barrier();
    if (w == 3) wr_v();
#pragma unroll
    for (int b = 0; b < 9; ++b)
#pragma unroll
      for (int tb = 0; tb < 2; ++tb) { pin_a(acc[b][tb]); Qp[b][tb] = u32x2{cvtpk(acc[b][tb][0], acc[b][tb][1]), cvtpk(acc[b][tb][2], acc[b][tb][3])}; }
#pragma unroll
    for (int b = 0; b < 9; ++b) pinu2(Qp[b][0], Qp[b][1]);
    u32x4 Vh[9];
    if (w > 0) {
#pragma unroll
      for (int b = 0; b < 9; ++b) { Kq[b] = rd128(ldsb + XBASE + (w - 1) * 9216 + b * 1024 + v16); undef_a(Vh[b]); }
    } else {
      const u32 hv = tile > 0 ? v16 : OOB;
      if (tile > 0) spin(v0, srdWS, WS_HFLAG + (tile * 4 + sq) * 128, (ep0 + 1) << 4, l == 0 ? 1u : 0u);
      const u32 hso = WS_HALO + (tile * 4 + sq) * 18432;
#pragma unroll
      for (int b = 0; b < 9; ++b) ld128_vs(Kq[b], hv, srdWS, hso + b * 1024);
#pragma unroll
      for (int b = 0; b < 9; ++b) ld128_a<0>(Vh[b], hv, srdWS, hso + 9216 + b * 1024);
    }
    wo_dma(IC(0)); wo_dma(IC(1)); wo_dma(IC(2));
    wait9<24>(Kq);
    STAMP(4);
    f32x4 cmc, cmp; u32 mcur[2];
    {
      const bool hasprev = tile > 0 || w > 0;
      u32 mb[4];
#pragma unroll
      for (int r = 0; r < 4; ++r) { const int i = 4 * g + r, q = tk; const bool vc = i <= q && i + 7 >= q, vp = hasprev && i >= q + 9;
        cmc[r] = vc ? 0.f : -1e30f; cmp[r] = vp ? 0.f : -1e30f; mb[r] = vc ? 0xffffu : 0u; }
      mcur[0] = mb[0] | (mb[1] << 16); mcur[1] = mb[2] | (mb[3] << 16);
    }
    s16x8 ones = {0x3f80, 0x3f80, 0x3f80, 0x3f80, 0x3f80, 0x3f80, 0x3f80, 0x3f80};
    const float sc = 0.23570226039551584f * 1.4426950408889634f;
    const u32 vr = ldsb + (16 * w + 4 * g + q4) * VROW + 8 * p4;
    f32x4 Sc[2][8], Sp[2][8], Ol[2][8], Oh[2][8], Sm[2][8];
    u32x4 Pf[2][8];
    s16x8 Vt[2][9];
    u32x2 qh[8], ql[8], Ob[2][9];
    auto qmask = [&](auto TB) { constexpr int tb = TB.value;
#pragma unroll
      for (int h = 0; h < 8; ++h) {
#pragma unroll
        for (int d = 0; d < 2; ++d) { qh[h][d] = (2 * g + d >= (u32)h) ? Qp[h][tb][d] : 0u; ql[h][d] = (2 * g + d <= (u32)h) ? Qp[h + 1][tb][d] : 0u; }
        pinu2(qh[h], ql[h]); }
      asm volatile("s_nop 1"); };
    auto phA = [&](auto TB, auto HP, auto J) { constexpr int tb = TB.value, j = J.value, h = 2 * HP.value + ((j >> 1) & 1);
      if constexpr (j < 4) { if constexpr (j & 1) mfma16_c(Sp[tb][h], u32x2{Kq[h][2 * tb], Kq[h][2 * tb + 1]}, qh[h], cmp); else mfma16_c(Sc[tb][h], u32x2{Kp[h][2 * tb], Kp[h][2 * tb + 1]}, qh[h], cmc); }
      else { if constexpr (j & 1) mfma16_v(Sp[tb][h], u32x2{Kq[h + 1][2 * tb], Kq[h + 1][2 * tb + 1]}, ql[h]); else mfma16_v(Sc[tb][h], u32x2{Kp[h + 1][2 * tb], Kp[h + 1][2 * tb + 1]}, ql[h]); } };
    f32x4 val[2]; float mx[2];
    auto phB = [&](auto TB, auto HP, auto I) { constexpr int tb = TB.value, i = I.value;
#pragma unroll
      for (int e = 0; e < 2; ++e) { const int h = 2 * HP.value + e;
        if constexpr (i == 0) { pin(Sc[tb][h]); pin(Sp[tb][h]);
#pragma unroll
          for (int r = 0; r < 4; ++r) val[e][r] = fmax2(Sc[tb][h][r], Sp[tb][h][r]);
          mx[e] = fmax2(fmax2(val[e][0], val[e][1]), fmax2(val[e][2], val[e][3])); }
        if constexpr (i == 1) {
          { auto s = __builtin_amdgcn_permlane16_swap(__float_as_uint(mx[e]), __float_as_uint(mx[e]), false, false); mx[e] = fmax2(__uint_as_float(s[0]), __uint_as_float(s[1])); }
          { auto s = __builtin_amdgcn_permlane32_swap(__float_as_uint(mx[e]), __float_as_uint(mx[e]), false, false); mx[e] = fmax2(__uint_as_float(s[0]), __uint_as_float(s[1])); }
          mx[e] = -mx[e] * sc; }
        if constexpr (i == 2) {
#pragma unroll
          for (int r = 0; r < 4; ++r) val[e][r] = __builtin_amdgcn_exp2f(__builtin_fmaf(val[e][r], sc, mx[e])); }
        if constexpr (i == 3) { const u32 k0 = cvtpk(val[e][0], val[e][1]), k1 = cvtpk(val[e][2], val[e][3]);
          Pf[tb][h] = u32x4{k0 & ~mcur[0], k1 & ~mcur[1], k0 & mcur[0], k1 & mcur[1]}; } }
      if constexpr (i < 2) pin2(mx[0], mx[1]);
      if constexpr (i == 2) { pin(val[0]); pin(val[1]); }
      if constexpr (i == 3) { pinu(Pf[tb][2 * HP.value]); pinu(Pf[tb][2 * HP.value + 1]); } };
    auto phC = [&](auto TB, auto HP, auto J) { constexpr int tb = TB.value, j = J.value, h = 2 * HP.value + j / 3, i = j % 3;
      if constexpr (j < 6) {
        if constexpr (i == 0) mfma_v0(Sm[tb][h], ones, Pf[tb][h]);
        if constexpr (i == 1) mfma_v0(Ol[tb][h], Vt[tb][h], Pf[tb][h]);
        if constexpr (i == 2) mfma_v0(Oh[tb][h], Vt[tb][h + 1], Pf[tb][h]); } };
    auto phD = [&](auto TB, auto HD) { constexpr int tb = TB.value, h = HD.value;
      pin(Sm[tb][h]); pin(Ol[tb][h]); pin(Oh[tb][h]);
      const float inv = __builtin_amdgcn_rcpf(Sm[tb][h][0]);
#pragma unroll
      for (int d = 0; d < 2; ++d) { const u32 lo = cvtpk(Ol[tb][h][2 * d] * inv, Ol[tb][h][2 * d + 1] * inv);
        if (h == 0) Ob[tb][0][d] = lo; else Ob[tb][h][d] = (2 * g + d < (u32)h) ? Ob[tb][h][d] : lo;
        Ob[tb][h + 1][d] = cvtpk(Oh[tb][h][2 * d] * inv, Oh[tb][h][2 * d + 1] * inv); }
      pinu2(Ob[tb][h], Ob[tb][h + 1]); };
    auto ld_vt = [&](auto TB) { constexpr int tb = TB.value;
      asm volatile("" ::: "memory");
#pragma unroll
      for (int b = 0; b < 9; ++b) Vt[tb][b] = cat(trrd(vr + tb * 80 * VROW + 32 * b), trrd(vr + (tb * 80 + 16) * VROW + 32 * b)); };
    asm volatile("" : "+v"(cmc), "+v"(cmp), "+v"(ones));
    qmask(IC(0));
    sfor<32>([&](auto J) { phA(IC(0), IC(J.value >> 3), IC(J.value & 7)); });
    qmask(IC(1));
    asm volatile("s_nop 7");
    sfor<16>([&](auto J) { constexpr int hp = J.value >> 2, i = J.value & 3; phA(IC(1), IC(hp), IC(2 * i)); phA(IC(1), IC(hp), IC(2 * i + 1)); phB(IC(0), IC(hp), IC(i)); });
    if (w == 0) {
      waitvm<15>();
#pragma unroll
      for (int b = 0; b < 9; ++b) { wr64_a(vw - 16 * VROW + 32 * b, Vh[b]); }
    }
    ld_vt(IC(0));
    asm volatile("s_nop 7" :: "v"(cmc), "v"(cmp));
    sfor<16>([&](auto J) { constexpr int hp = J.value >> 2, i = J.value & 3; phC(IC(0), IC(hp), IC(2 * i)); phC(IC(0), IC(hp), IC(2 * i + 1)); phB(IC(1), IC(hp), IC(i)); });
    ld_vt(IC(1));
    asm volatile("s_nop 7");
    sfor<8>([&](auto J) { constexpr int h = J.value; phC(IC(1), IC(h >> 1), IC(3 * (h & 1))); phC(IC(1), IC(h >> 1), IC(3 * (h & 1) + 1)); phC(IC(1), IC(h >> 1), IC(3 * (h & 1) + 2)); phD(IC(0), J); });
    asm volatile("s_nop 7");
    sfor<8>([&](auto J) { phD(IC(1), J); });
    STAMP(5);
    asm volatile("s_waitcnt vmcnt(0) lgkmcnt(0)" ::: "memory");
    __builtin_amdgcn_s_barrier();
    u32x4 OB[4][2];
#pragma unroll
    for (int j = 0; j < 4; ++j)
#pragma unroll
      for (int tb = 0; tb < 2; ++tb) { OB[j][tb] = u32x4{Ob[tb][2 * j][0], Ob[tb][2 * j][1], Ob[tb][2 * j + 1][0], Ob[tb][2 * j + 1][1]}; pinu(OB[j][tb]); }
    pinu2(Ob[0][8], Ob[1][8]);
    u32 wb[3][4];
#pragma unroll
    for (int u = 0; u < 4; ++u) { wb[0][u] = ldsb + hh * 1024 + 128 * rr + 32 * (u ^ (rr >> 1)) + 8 * p4; wb[1][u] = wb[0][u] + 57344; wb[2][u] = wb[0][u] + XBASE; }
    const u32 tag = (ep0 + 1) << 4, fso = WS_FLAG + (tile * 4 + w) * 128 + (12 + sq) * 4;
    const u32 sbase = WS_P + ((tile * 9 * 8 + 4 + sq) * 4 + w) * 4096;
    f32x4 as[4][2];
    u32x4 sd[4];
    s16x4 Wl[12], Wh[12];
    auto wrd = [&](auto FI, auto T) { constexpr int f = FI.value, G = f / 20, k = (f % 20) >> 2, u = f & 3;
      if constexpr (G < 9) { constexpr int so = wslot(G), bs = so >= (int)XBASE ? 2 : so >= 57344 ? 1 : 0, rel = so - (bs == 2 ? (int)XBASE : bs == 1 ? 57344 : 0);
        if constexpr (T.value == 0) FR<f % 12>::template lo<rel + k * 4096>(Wl[f % 12], wb[bs][u]);
        else if constexpr (k < 4) FR<f % 12>::template hi<rel + k * 4096 + 2048>(Wh[f % 12], wb[bs][u]); } };
    auto cvt_s = [&](auto I) { constexpr int u = I.value >> 1, tb = I.value & 1; sd[u][2 * tb] = v_cvtf(as[u][tb][0], as[u][tb][1]); sd[u][2 * tb + 1] = v_cvtf(as[u][tb][2], as[u][tb][3]); };
    sfor<8>([&](auto F) { wrd(F, IC(0)); wrd(F, IC(1)); });
    sfor<9>([&](auto GG) { constexpr int G = GG.value;
      if constexpr (G >= 1) cvt_s(IC(0));
      sfor<40>([&](auto J) { constexpr int j = J.value, k = j >> 3, u = (j >> 1) & 3, tb = j & 1, f = 20 * G + (j >> 1);
        if constexpr (j == 30 && G >= 4 && G != 5) { waitvm<4>(); st_flag(tag + G - 1, srdWS, v0, fso); }
        if constexpr (j == 24 && G == 2) waitvm<16>();
        if constexpr (j == 24 && G == 5) { waitvm<3>(); st_flag(tag + 4, srdWS, v0, fso); }
        if constexpr ((j == 0 && G >= 1 && G <= 3) || (j == 24 && (G == 2 || G == 5))) __builtin_amdgcn_s_barrier();
        if constexpr (j % 8 == 0) wl<(G == 8 && k == 4) ? 0 : (k == 3 ? 4 : 8)>();
        if constexpr (k == 0) FR<f % 12>::vv0(as[u][tb], Wl[f % 12], Wh[f % 12], OB[0][tb]);
        else if constexpr (k < 4) FR<f % 12>::vv(as[u][tb], Wl[f % 12], Wh[f % 12], OB[k][tb]);
        else FR<f % 12>::k16(as[u][tb], Wl[f % 12], Ob[tb][8]);
        wrd(IC(f + 8), IC(tb));
        if constexpr (G == 0 && j < 30 && (j & 1)) wo_piece(IC(3 + j / 10), IC((j % 10) >> 1));
        if constexpr (G >= 1 && G <= 3 && j % 6 == 3 && j < 30) wo_piece(IC(G + 5), IC(j / 6));
        if constexpr (G >= 1 && j >= 6 && j <= 24 && j % 6 == 0) st128(sd[j / 6 - 1], srdWS, v16 + (j / 6 - 1) * 1024, sbase + (G - 1) * 131072);
        if constexpr (G >= 1 && j == 26) keep4(sd[0], sd[1], sd[2], sd[3]);
        SB0();
        if constexpr (G >= 1 && j + 1 < 8) cvt_s(IC(j + 1));
        SB0();
      });
    });
    asm volatile("s_nop 7");
    sfor<8>(cvt_s);
    asm volatile("s_nop 0");
    sfor<4>([&](auto U) { st128(sd[U.value], srdWS, v16 + U.value * 1024, sbase + 8 * 131072); });
    waitvm<0>();
    st_flag(tag + 9, srdWS, v0, fso);
    STAMP(6); STAMP(7);
    STAMP_FLUSH();
    asm volatile("fused_code_end:");
  }
}

extern "C" void kernel_launch(void* const* d_in, const int* in_sizes, int n_in, void* d_out, int out_size, void* d_ws, size_t ws_size, hipStream_t stream) {
  (void)hipFuncSetAttribute((const void*)fused, hipFuncAttributeMaxDynamicSharedMemorySize, LDS_BYTES);
  fused<<<256, 256, LDS_BYTES, stream>>>((const u16*)d_in[0], (const u16*)d_in[1], (const u16*)d_in[2], (const u16*)d_in[4], (const u16*)d_in[5], (u16*)d_out, (char*)d_ws);
}
```

```cpp
#include <hip/hip_runtime.h>
#include <utility>
#include <type_traits>
#define XROT 0
#define ABL 0
#define NFIRST 15
#define PAUX "sc1"
#define TLBW 1
#define GRAY 0
typedef unsigned short u16;
typedef unsigned u32;
typedef short s16x2 __attribute__((ext_vector_type(2)));
typedef short s16x4 __attribute__((ext_vector_type(4)));
typedef short s16x8 __attribute__((ext_vector_type(8)));
typedef __bf16 bf16x8 __attribute__((ext_vector_type(8)));
typedef __bf16 bf16x2 __attribute__((ext_vector_type(2)));
typedef float f32x2 __attribute__((ext_vector_type(2)));
typedef float f32x4 __attribute__((ext_vector_type(4)));
typedef u32 u32x2 __attribute__((ext_vector_type(2)));
typedef u32 u32x4 __attribute__((ext_vector_type(4)));
#define LDS3 __attribute__((address_space(3)))
#define DI __device__ __forceinline__
#define SB0() __builtin_amdgcn_sched_barrier(0)
#define IC(x) std::integral_constant<int, (x)>{}
template <int... Is, class F> DI void sfor_impl(std::integer_sequence<int, Is...>, F&& f) { (f(std::integral_constant<int, Is>{}), ...); }
template <int N, class F> DI void sfor(F&& f) { sfor_impl(std::make_integer_sequence<int, N>{}, f); }
typedef __amdgpu_buffer_rsrc_t srd_t;

constexpr int SEQ = 2048, DM = 576, DFF = 4608;
constexpr u32 XROW = 1152, W1ROW = 9216, WQROW = 3456, BIAS = 4096, OOB = 0x80000000u;
constexpr u32 WS_TILE = 4u << 20;
constexpr u32 WS_EPOCH = 0;
constexpr u32 WS_FLAG = 256;
constexpr u32 WS_HFLAG = 1024;
constexpr u32 WS_HALO = 4096;
constexpr u32 WS_P = 128u << 10;
constexpr u32 WS_H = 2u << 20;
constexpr u32 WS_END = 16 * WS_TILE;
constexpr u32 WS_STAMP = 64u << 20;
constexpr u32 FSLOT = 24576, FNS = 5, ASLOT = 28672, ANS = 4, XBASE = 122880, TBASE = 155648, SBASE = 156672, LDS_BYTES = 157696;
constexpr u32 VROW = 296;
constexpr u32 SPIN_MAX = 1u << 16;

DI srd_t mksrd(const void* p, u32 n) { return __builtin_amdgcn_make_buffer_rsrc((void*)p, (short)0, (int)n, 0x00020000); }
DI srd_t mksrd_b(const void* p, u32 n) { return mksrd((const char*)p - BIAS, n + BIAS); }
DI void setm0(u32 v) { asm volatile("s_mov_b32 m0, %0\n\ts_nop 0" :: "s"(v) : "memory"); }
template <int B> DI void setm0_add(u32 a) { asm volatile("s_add_u32 m0, %0, %1" :: "s"(a), "i"(B) : "memory", "scc"); }
template <int OFF> DI void dma16(u32 voff, srd_t srd, u32 soff) {
  asm volatile("buffer_load_dwordx4 %0, %1, %2 offen offset:%3 lds" :: "v"(voff), "s"(srd), "s"(soff), "i"(OFF) : "memory");
}
template <int OFF> DI void ld128_asm(u32x4& d, u32 voff, srd_t srd, u32 soff) {
  asm volatile("buffer_load_dwordx4 %0, %1, %2, %3 offen offset:%4" : "=v"(d) : "v"(voff), "s"(srd), "s"(soff), "i"(OFF) : "memory");
}
DI void touch(u32 voff, srd_t srd, u32 soff) { if (ABL & 1) voff = OOB; asm volatile("buffer_load_dword %0, %1, %2 offen lds" :: "v"(voff), "s"(srd), "s"(soff) : "memory"); }
DI void poll_issue(u32& d, u32 voff, srd_t srd, u32 soff) { asm volatile("buffer_load_dword %0, %1, %2, %3 offen sc1" : "=v"(d) : "v"(voff), "s"(srd), "s"(soff) : "memory"); }
template <int N> DI void waitvm() { asm volatile("s_waitcnt vmcnt(%0)" :: "i"(N) : "memory"); }
template <int N> DI void waitvm_v(u32& d) { asm volatile("s_waitcnt vmcnt(%1)" : "+v"(d) : "i"(N) : "memory"); }
template <int N> DI void wait9(u32x4 (&p)[9]) {
  asm volatile("s_waitcnt vmcnt(%9)" : "+v"(p[0]), "+v"(p[1]), "+v"(p[2]), "+v"(p[3]), "+v"(p[4]), "+v"(p[5]), "+v"(p[6]), "+v"(p[7]), "+v"(p[8]) : "i"(N) : "memory");
}
template <int N> DI void waitlgkm() { __builtin_amdgcn_s_waitcnt(0xC07F | (N << 8)); }
DI s16x4 trrd(u32 a) { return __builtin_amdgcn_ds_read_tr16_b64_v4i16((LDS3 s16x4*)(size_t)a); }
DI u32x4 rd128(u32 a) { return *(LDS3 u32x4*)(size_t)a; }
DI void wr128(u32 a, u32x4 v) { *(LDS3 u32x4*)(size_t)a = v; }
DI void wr64(u32 a, u32x2 v) { *(LDS3 u32x2*)(size_t)a = v; }
DI u32 cvtpk(float lo, float hi) { f32x2 v = {lo, hi}; return __builtin_bit_cast(u32, __builtin_convertvector(v, bf16x2)); }
DI float bflo(u32 d) { return __uint_as_float(d << 16); }
DI float bfhi(u32 d) { return __uint_as_float(d & 0xffff0000u); }
DI float rbf(float f) { return bflo(cvtpk(f, 0.f)); }
DI float dot2(u32 a, u32 b, float c) { return __builtin_amdgcn_fdot2_f32_bf16(__builtin_bit_cast(bf16x2, a), __builtin_bit_cast(bf16x2, b), c, false); }
DI s16x8 cat(s16x4 lo, s16x4 hi) { return __builtin_shufflevector(lo, hi, 0, 1, 2, 3, 4, 5, 6, 7); }
DI f32x4 mfma(s16x8 a, u32x4 b, f32x4 c) { return __builtin_amdgcn_mfma_f32_16x16x32_bf16(__builtin_bit_cast(bf16x8, a), __builtin_bit_cast(bf16x8, b), c, 0, 0, 0); }
DI void mfma_v(f32x4& c, s16x8 a, u32x4 b) { asm volatile("v_mfma_f32_16x16x32_bf16 %0, %1, %2, %0" : "+v"(c) : "v"(a), "v"(b)); }
DI void mfma_v0(f32x4& c, s16x8 a, u32x4 b) { asm volatile("v_mfma_f32_16x16x32_bf16 %0, %1, %2, 0" : "=&v"(c) : "v"(a), "v"(b)); }
DI void mfma_a(f32x4& c, s16x8 a, u32x4 b) { asm volatile("v_mfma_f32_16x16x32_bf16 %0, %1, %2, %0" : "+v"(c) : "v"(a), "a"(b)); }
DI void mfma_a0(f32x4& c, s16x8 a, u32x4 b) { asm volatile("v_mfma_f32_16x16x32_bf16 %0, %1, %2, 0" : "=&v"(c) : "v"(a), "a"(b)); }
DI void mfma_acc(f32x4& c, s16x8 a, u32x4 b) { asm volatile("v_mfma_f32_16x16x32_bf16 %0, %1, %2, %0" : "+a"(c) : "v"(a), "v"(b)); }
DI void mfma_acc_a(f32x4& c, s16x8 a, u32x4 b) { asm volatile("v_mfma_f32_16x16x32_bf16 %0, %1, %2, %0" : "+a"(c) : "a"(a), "v"(b)); }
DI void pin_a(f32x4& a) { asm volatile("" : "+a"(a)); }
DI void mfma_av(f32x4& c, s16x8 a, u32x4 b) { asm volatile("v_mfma_f32_16x16x32_bf16 %0, %1, %2, %0" : "+v"(c) : "a"(a), "v"(b)); }
DI void mfma_av0(f32x4& c, s16x8 a, u32x4 b) { asm volatile("v_mfma_f32_16x16x32_bf16 %0, %1, %2, 0" : "=&v"(c) : "a"(a), "v"(b)); }
DI void mfma_aa(f32x4& c, s16x8 a, u32x4 b) { asm volatile("v_mfma_f32_16x16x32_bf16 %0, %1, %2, %0" : "+v"(c) : "a"(a), "a"(b)); }
DI void mfma_aa0(f32x4& c, s16x8 a, u32x4 b) { asm volatile("v_mfma_f32_16x16x32_bf16 %0, %1, %2, 0" : "=&v"(c) : "a"(a), "a"(b)); }
template <int OFF> DI void trrd_a(s16x4& d, u32 a) { asm volatile("ds_read_b64_tr_b16 %0, %1 offset:%2" : "=a"(d) : "v"(a), "i"(OFF) : "memory"); }
template <int OFF> DI void trrd_v(s16x4& d, u32 a) { asm volatile("ds_read_b64_tr_b16 %0, %1 offset:%2" : "=v"(d) : "v"(a), "i"(OFF) : "memory"); }
template <int OFF> DI void rd128_v(u32x4& d, u32 a) { asm volatile("ds_read_b128 %0, %1 offset:%2" : "=v"(d) : "v"(a), "i"(OFF) : "memory"); }
template <int OFF> DI void ld128_a(u32x4& d, u32 voff, srd_t srd, u32 soff) {
  asm volatile("buffer_load_dwordx4 %0, %1, %2, %3 offen offset:%4 " PAUX : "=a"(d) : "v"(voff), "s"(srd), "s"(soff), "i"(OFF) : "memory"); }
template <int N> DI void wl() { asm volatile("s_waitcnt lgkmcnt(%0)" :: "i"(N) : "memory"); }
DI void zero_v(f32x4& a) { f32x2 lo, hi; asm volatile("v_mov_b64 %0, 0\n\tv_mov_b64 %1, 0" : "=v"(lo), "=v"(hi)); a = __builtin_shufflevector(lo, hi, 0, 1, 2, 3); }
DI float fmax2(float a, float b) { return __builtin_elementwise_maximum(a, b); }
template <int R> struct FR;
#define DEF_FR(R, A0, A1, A2, A3) template <> struct FR<R> { \
  template <int OFF> static DI void lo(s16x4& d, u32 a) { asm volatile("ds_read_b64_tr_b16 %0, %1 offset:%2" : "={a[" #A0 ":" #A1 "]}"(d) : "v"(a), "i"(OFF) : "memory"); } \
  template <int OFF> static DI void hi(s16x4& d, u32 a) { asm volatile("ds_read_b64_tr_b16 %0, %1 offset:%2" : "={a[" #A2 ":" #A3 "]}"(d) : "v"(a), "i"(OFF) : "memory"); } \
  static DI void vv(f32x4& c, s16x4 l, s16x4 h, u32x4 b) { asm volatile("v_mfma_f32_16x16x32_bf16 %0, a[" #A0 ":" #A3 "], %3, %0" : "+v"(c) : "{a[" #A0 ":" #A1 "]}"(l), "{a[" #A2 ":" #A3 "]}"(h), "v"(b)); } \
  static DI void vv0(f32x4& c, s16x4 l, s16x4 h, u32x4 b) { asm volatile("v_mfma_f32_16x16x32_bf16 %0, a[" #A0 ":" #A3 "], %3, 0" : "=&v"(c) : "{a[" #A0 ":" #A1 "]}"(l), "{a[" #A2 ":" #A3 "]}"(h), "v"(b)); } \
  static DI void va(f32x4& c, s16x4 l, s16x4 h, u32x4 b) { asm volatile("v_mfma_f32_16x16x32_bf16 %0, a[" #A0 ":" #A3 "], %3, %0" : "+v"(c) : "{a[" #A0 ":" #A1 "]}"(l), "{a[" #A2 ":" #A3 "]}"(h), "a"(b)); } \
  static DI void va0(f32x4& c, s16x4 l, s16x4 h, u32x4 b) { asm volatile("v_mfma_f32_16x16x32_bf16 %0, a[" #A0 ":" #A3 "], %3, 0" : "=&v"(c) : "{a[" #A0 ":" #A1 "]}"(l), "{a[" #A2 ":" #A3 "]}"(h), "a"(b)); } \
  static DI void k16(f32x4& c, s16x4 l, u32x2 b) { asm volatile("v_mfma_f32_16x16x16_bf16 %0, a[" #A0 ":" #A1 "], %2, %0" : "+v"(c) : "{a[" #A0 ":" #A1 "]}"(l), "v"(b)); } \
  static DI void av(f32x4& c, s16x4 l, s16x4 h, u32x4 b) { asm volatile("v_mfma_f32_16x16x32_bf16 %0, a[" #A0 ":" #A3 "], %3, %0" : "+a"(c) : "{a[" #A0 ":" #A1 "]}"(l), "{a[" #A2 ":" #A3 "]}"(h), "v"(b)); } };
DEF_FR(0, 208, 209, 210, 211)
DEF_FR(1, 212, 213, 214, 215)
DEF_FR(2, 216, 217, 218, 219)
DEF_FR(3, 220, 221, 222, 223)
DEF_FR(4, 224, 225, 226, 227)
DEF_FR(5, 228, 229, 230, 231)
DEF_FR(6, 232, 233, 234, 235)
DEF_FR(7, 236, 237, 238, 239)
DEF_FR(8, 240, 241, 242, 243)
DEF_FR(9, 244, 245, 246, 247)
DEF_FR(10, 248, 249, 250, 251)
DEF_FR(11, 252, 253, 254, 255)
DI void mfma_vn(f32x4& c, s16x8 a, u32x4 b) { asm volatile("s_nop 1\n\tv_mfma_f32_16x16x32_bf16 %0, %1, %2, %0" : "+v"(c) : "v"(a), "v"(b)); }
DI void mfma_v0n(f32x4& c, s16x8 a, u32x4 b) { asm volatile("s_nop 1\n\tv_mfma_f32_16x16x32_bf16 %0, %1, %2, 0" : "=&v"(c) : "v"(a), "v"(b)); }
DI void mfma16_c(f32x4& d, u32x2 a, u32x2 b, f32x4 c) { asm volatile("v_mfma_f32_16x16x16_bf16 %0, %1, %2, %3" : "=&v"(d) : "v"(a), "v"(b), "v"(c)); }
DI void mfma16_v(f32x4& d, u32x2 a, u32x2 b) { asm volatile("v_mfma_f32_16x16x16_bf16 %0, %1, %2, %0" : "+v"(d) : "v"(a), "v"(b)); }
template <int CTRL> DI float dppadd(float v) { return v + __int_as_float(__builtin_amdgcn_update_dpp(0, __float_as_int(v), CTRL, 0xf, 0xf, true)); }
DI u32 v_lshl16(u32 a) { u32 r; asm volatile("v_lshlrev_b32 %0, 16, %1" : "=v"(r) : "v"(a)); return r; }
DI u32 v_andhi(u32 a) { u32 r; asm volatile("v_and_b32 %0, 0xffff0000, %1" : "=v"(r) : "v"(a)); return r; }
DI u32 v_fmav(u32 a, float b, float c) { u32 r; asm volatile("v_fma_f32 %0, %1, %2, %3" : "=v"(r) : "v"(a), "v"(b), "v"(c)); return r; }
DI u32 v_cvtv(u32 a, u32 b) { u32 r; asm volatile("v_cvt_pk_bf16_f32 %0, %1, %2" : "=v"(r) : "v"(a), "v"(b)); return r; }
DI u32 v_cvtf(float a, float b) { u32 r; asm volatile("v_cvt_pk_bf16_f32 %0, %1, %2" : "=v"(r) : "v"(a), "v"(b)); return r; }
DI u32 v_addv(u32 a, u32 s) { u32 r; asm volatile("v_add_u32 %0, %1, %2" : "=v"(r) : "s"(s), "v"(a)); return r; }
DI void v_swap(u32& a, u32& b) { asm volatile("v_swap_b32 %0, %1" : "+v"(a), "+v"(b)); }
DI void keep4(u32x4 a, u32x4 b, u32x4 c, u32x4 d) { asm volatile("" :: "v"(a), "v"(b), "v"(c), "v"(d)); }
DI void pin(f32x4& a) { asm volatile("" : "+v"(a)); }
DI void pin2(float& a, float& b) { asm volatile("" : "+v"(a), "+v"(b)); }
DI void zero_a(f32x4& a) { a = f32x4{0, 0, 0, 0}; asm volatile("" : "+a"(a)); }
DI u32 sopaque(u32 v) { asm volatile("" : "+s"(v)); return v; }
DI float max2(float a, float b) { float r; asm("v_max_f32 %0, %1, %2" : "=v"(r) : "v"(a), "v"(b)); return r; }
DI float max3(float a, float b, float c) { float r; asm("v_max3_f32 %0, %1, %2, %3" : "=v"(r) : "v"(a), "v"(b), "v"(c)); return r; }
DI void pin1(float& a) { asm volatile("" : "+v"(a)); }
DI void pinu(u32x4& a) { asm volatile("" : "+v"(a)); }
DI void pinu2(u32x2& a, u32x2& b) { asm volatile("" : "+v"(a), "+v"(b)); }
DI void undef_v(f32x4& a) { asm volatile("" : "=v"(a)); }
DI void undef_a(u32x4& a) { asm volatile("" : "=a"(a)); }
DI void pinw(u32& a) { asm volatile("" : "+v"(a)); }
DI void wr64_a(u32 a, u32x4 d) {
  asm volatile("ds_write_b64 %0, %1\n\tds_write_b64 %0, %2 offset:%3" :: "v"(a), "a"(__builtin_shufflevector(d, d, 0, 1)), "a"(__builtin_shufflevector(d, d, 2, 3)), "i"(80 * VROW) : "memory"); }
DI void mfma2_v(f32x4& c, s16x4 l, s16x4 h, u32x4 b) { mfma_v(c, cat(l, h), b); }
DI void mfma2_v0(f32x4& c, s16x4 l, s16x4 h, u32x4 b) { mfma_v0(c, cat(l, h), b); }
DI void ld128_vs(u32x4& d, u32 voff, srd_t srd, u32 soff) { asm volatile("buffer_load_dwordx4 %0, %1, %2, %3 offen sc1" : "=v"(d) : "v"(voff), "s"(srd), "s"(soff) : "memory"); }
DI void ld64_v(u32x2& d, u32 voff, srd_t srd, u32 soff) { asm volatile("buffer_load_dwordx2 %0, %1, %2, %3 offen" : "=v"(d) : "v"(voff), "s"(srd), "s"(soff) : "memory"); }
DI void pinw_a(u32& a) { asm volatile("" : "+a"(a)); }
DI u32x2 rd64(u32 a) { return *(LDS3 u32x2*)(size_t)a; }
DI void pins(s16x8& a) { asm volatile("" : "+v"(a)); }
DI void nop8(f32x4 (&a)[4][2]) { asm volatile("s_nop 7" : "+v"(a[0][0]), "+v"(a[0][1]), "+v"(a[1][0]), "+v"(a[1][1]), "+v"(a[2][0]), "+v"(a[2][1]), "+v"(a[3][0]), "+v"(a[3][1])); }
DI srd_t code_srd(int& last) {
  unsigned long long pc; int len;
  asm volatile("s_getpc_b64 %0\n1:\n\ts_mov_b32 %1, fused_code_end-1b" : "=s"(pc), "=s"(len));
  last = len > 2048 ? len - 2048 : 0;
  return mksrd((const void*)pc, len > 2048 ? (u32)len : 0u);
}
template <int OFF> DI void tlb_touch(u32& d, const void* p) { asm volatile("s_load_dword %0, %1, %2" : "+s"(d) : "s"(p), "i"(OFF) : "memory"); }
DI void tlb_done(u32& d) { asm volatile("s_waitcnt lgkmcnt(0)" : "+s"(d) :: "memory"); }
DI u32 rfl(u32 v) { return __builtin_amdgcn_readfirstlane(v); }
DI void st128(u32x4 d, srd_t s, u32 v, u32 so) { if (ABL & 4) v = OOB; if (ABL & 8) { __builtin_amdgcn_raw_buffer_store_b128(d, s, v, so, 0); return; } __builtin_amdgcn_raw_buffer_store_b128(d, s, v, so, 16); }
DI u32x4 ld128(srd_t s, u32 v, u32 so) { return __builtin_amdgcn_raw_buffer_load_b128(s, v, so, 16); }
DI void st_flag(u32 val, srd_t s, u32 v0, u32 so) { __builtin_amdgcn_raw_buffer_store_b32(val, s, v0, so, 16); }
DI bool flags_ok(u32 word, u32 tag, u32 need) { const u32 d = word - tag; return __all(need == 0 || (d < 16u && d >= need)); }
DI void spin(u32 voff, srd_t s, u32 so, u32 tag, u32 need) {
#pragma unroll 1
  for (u32 i = 0; i < SPIN_MAX; ++i) { u32 wd; poll_issue(wd, voff, s, so); waitvm_v<0>(wd); if (flags_ok(wd, tag, need)) break; __builtin_amdgcn_s_sleep(1); }
}
DI void spin2(u32 voff, srd_t s, u32 so, u32 lo, u32 lim) {
#pragma unroll 1
  for (u32 i = 0; i < SPIN_MAX; ++i) { __builtin_amdgcn_s_sleep(1); u32 wd; poll_issue(wd, voff, s, so); waitvm_v<0>(wd); if (__all(wd - lo <= lim)) break; }
}
DI void spin2n(u32 voff, srd_t s, u32 so, u32 lo, u32 lim) {
#pragma unroll 1
  for (u32 i = 0; i < SPIN_MAX; ++i) { u32 wd; poll_issue(wd, voff, s, so); waitvm_v<0>(wd); if (__all(wd - lo <= lim)) break; __builtin_amdgcn_s_sleep(1); }
}
#define STAMP(i)
#define STAMP_DECL
#define STAMP_FLUSH()

enum { OP_W = 1, OP_HS, OP_HB, OP_HC, OP_FS, OP_FLAG, OP_POLL, OP_MISC, OP_SL, OP_PL };
constexpr int dpos(int m) { return m == 3 ? 0 : m == 9 ? 1 : m == 15 ? 2 : m == 21 ? 3 : m == 27 ? 4 : m == 30 ? 5 : -1; }
constexpr int fpos(int m) { return (m == 6 || m == 12 || m == 18 || m == 24) ? m / 6 - 1 : (m >= 35 && m % 4 == 3) ? 4 + (m - 35) / 4 : -1; }
constexpr int f2op(bool O, int J, int m) {
  if (J <= 14 && dpos(m) >= 0) return OP_W;
  if (J == -2) return (m == 34 || m == 37 || m == 40 || m == 43 || m == 46) ? OP_MISC : 0;
  if (J == -1) return m == 46 ? OP_MISC : 0;
  const int p = fpos(m);
  if (J <= 5 && (p == 0 || p == 2 || p == 5)) return OP_HS;
  if ((J == 2 && m == 47) || (J == 6 && m == 47) || (J == 15 && m == 12) || (J == 17 && m == 10)) return OP_FLAG;
  if ((J == 3 && m == 43) || (J == 7 && m == 47)) return OP_POLL;
  if (O && ((J == 12 && m == 33) || (J == 15 && m == 44) || (J == 17 && m == 40))) return OP_POLL;
  if ((J == 4 && (p == 4 || p == 6 || p == 7) ) || (J == 4 && m == 33) || (J == 5 && (p == 1 || p == 3 || p == 4 || p == 6 || p == 7 || m == 1 || m == 33 || m == 45))) return OP_HB;
  if ((J == 6 && p >= 0 && p < 7) || (J == 7 && p >= 0 && p < 5)) return OP_HC;
  if ((J == 8 && p >= 4) || (J == 9 && p >= 0)) return OP_HB;
  if ((J == 10 && p >= 0) || (J == 11 && p >= 0 && p < 4)) return OP_HC;
  if ((J == 14 || J == 16) && (m == 2 || m == 5 || m == 8 || m == 11)) return OP_FS;
  if (O && ((J == 13 && p >= 4) || (J == 14 && (p >= 2 || m == 33)) || (J == 15 && m >= 15 && m < 30 && m % 3 == 0))) return OP_SL;
  if (O && J == 17 && m >= 13 && m < 37 && m % 3 == 1) return OP_PL;
  if (O && J == 16 && m >= 16 && m < 32 && m % 4 == 0) return OP_MISC;
  return 0;
}
constexpr int f2count(bool O, int J0, int m0, int J1, int m1) {
  int n = 0;
  for (int J = J0; J <= J1; ++J) for (int m = 0; m < 48; ++m) { if (J == J0 && m <= m0) continue; if (J == J1 && m >= m1) continue; n += f2op(O, J, m) != 0; }
  if (J0 < 0 && J1 >= 0) n += 6;
  return n > 63 ? 63 : n;
}
constexpr int f2index(bool O, int J, int m) {
  int n = 0; const int k = f2op(O, J, m);
  for (int j = 0; j <= J; ++j) for (int q = 0; q < 48; ++q) { if (j == J && q >= m) break; n += f2op(O, j, q) == k; }
  return n;
}
constexpr int f2find(bool O, int k, int idx) {
  int n = 0;
  for (int j = 0; j < 18; ++j) for (int q = 0; q < 48; ++q) if (f2op(O, j, q) == k) { if (n == idx) return 48 * j + q; ++n; }
  return -1;
}
constexpr int f2after(bool O, int k, int idx, int J, int m) { return f2count(O, f2find(O, k, idx) / 48, f2find(O, k, idx) % 48, J, m); }
constexpr int imin(int a, int b) { return a < b ? a : b; }
static_assert(f2find(1, OP_HS, 17) >= 0 && f2find(1, OP_HS, 18) < 0 && f2find(1, OP_SL, 15) >= 0 && f2find(1, OP_SL, 16) < 0 && f2find(1, OP_PL, 7) >= 0 && f2find(1, OP_PL, 8) < 0 &&
              f2find(1, OP_HB, 23) >= 0 && f2find(1, OP_HB, 24) < 0 && f2find(1, OP_HC, 23) >= 0 && f2find(1, OP_HC, 24) < 0 && f2find(1, OP_FS, 7) >= 0, "schedule");

struct XState { u32 vx[4]; srd_t srd; float rs[2], sh[2]; u32 bx; };
DI void x_dma(const XState& X, u32 ldsdst, u32 soff) {
  setm0(ldsdst);
  sfor<4>([&](auto P) { dma16<P.value * 1024>(X.vx[P.value], X.srd, soff); });
}
DI u32x4 norm_frag(u32x4 r, float rs, float sh) {
  u32x4 o;
#pragma unroll
  for (int i = 0; i < 4; ++i) o[i] = cvtpk(__builtin_fmaf(bflo(r[i]), rs, sh), __builtin_fmaf(bfhi(r[i]), rs, sh));
  return o;
}
template <int K> DI void norm_op(const XState& X, const u32x4 (&raw)[2], u32x4 (&Bn)[2], u32 (&tlo)[2], u32 (&thi)[2]) {
  constexpr int blk = K / 10, r = K % 10, i = 2 * blk + (r & 1), tb = i >> 2, d = i & 3, op = r >> 1, z = r & 1;
  if constexpr (op == 0) tlo[z] = v_lshl16(raw[tb][d]);
  if constexpr (op == 1) thi[z] = v_andhi(raw[tb][d]);
  if constexpr (op == 2) tlo[z] = v_fmav(tlo[z], X.rs[tb], X.sh[tb]);
  if constexpr (op == 3) thi[z] = v_fmav(thi[z], X.rs[tb], X.sh[tb]);
  if constexpr (op == 4) Bn[tb][d] = v_cvtv(tlo[z], thi[z]);
}
template <int N> DI void ln_group(u32x4 (&p)[9], u32 dst, u32 xw) {
  wait9<N>(p);
  wr128(xw, p[0]); wr128(xw + 4096, p[1]);
  float a0 = 0, a1 = 0, b0 = 0, b1 = 0;
#pragma unroll
  for (int c = 0; c < 9; ++c) { a0 = dot2(p[c][0], 0x3f803f80u, a0); a1 = dot2(p[c][1], 0x3f803f80u, a1); a0 = dot2(p[c][2], 0x3f803f80u, a0); a1 = dot2(p[c][3], 0x3f803f80u, a1);
    b0 = dot2(p[c][0], p[c][0], b0); b1 = dot2(p[c][1], p[c][1], b1); b0 = dot2(p[c][2], p[c][2], b0); b1 = dot2(p[c][3], p[c][3], b1); }
  const float a = dppadd<0x141>(dppadd<0x4E>(dppadd<0xB1>(a0 + a1))), b = dppadd<0x141>(dppadd<0x4E>(dppadd<0xB1>(b0 + b1)));
  const float m = a * (1.f / 576.f), var = fmaxf(b * (1.f / 576.f) - m * m, 0.f);
  const float rs = __builtin_amdgcn_rcpf(rbf(__builtin_amdgcn_sqrtf(rbf(rbf(var) + 1e-5f))));
  wr64(dst, u32x2{__float_as_uint(rs), __float_as_uint(-rbf(m) * rs)});
  asm volatile("" ::: "memory");
}
DI void epoch_issue(u32& e, const char* p) { asm volatile("s_load_dword %0, %1, 0x0" : "=s"(e) : "s"(p) : "memory"); }
DI void epoch_wait(u32& e0, u32& e1) { asm volatile("s_waitcnt lgkmcnt(0)" : "+s"(e0), "+s"(e1) :: "memory"); }

__global__ __launch_bounds__(256, 1) __attribute__((flatten)) void fused(const u16* __restrict__ x, const u16* __restrict__ wqkv, const u16* __restrict__ wo, const u16* __restrict__ w1,
                                               const u16* __restrict__ w2, u16* __restrict__ out, char* __restrict__ ws) {
  extern __shared__ __attribute__((aligned(16))) char smem[];
  STAMP_DECL;
  STAMP(0);
  const u32 ldsb = (u32)(size_t)(LDS3 char*)smem;
  const u32 l = threadIdx.x & 63, w = rfl(threadIdx.x >> 6);
  const u32 bid = blockIdx.x, xcd = bid & 7, jj = bid >> 3;
  const u32 t7 = jj & 7, tile = (xcd & 1) * 8 + t7, sq = xcd >> 1, role = jj >> 3;
  const u32 tok0 = tile * 128;
  const u32 r8 = l >> 3, s8 = l & 7;
  const u32 g = l >> 4, tk = l & 15, q4 = (l >> 2) & 3, p4 = l & 3, rr = 4 * (g & 1) + q4, hh = g >> 1;
  XState X;
  X.srd = mksrd_b(x, SEQ * XROW);
  const u32 pa0 = BIAS + (tok0 + 32 * w + 2 * r8) * XROW + 16 * s8;
  auto grp_so = [&](int i) { const u32 p = (i + XROT * role) & 3; return (16 * (p & 1) + (p >> 1)) * XROW; };
  auto ld_part = [&](u32x4 (&p)[9], int i, auto LO, auto N) { const u32 so = grp_so(i); sfor<N.value>([&](auto C) { constexpr int c = LO.value + C.value; ld128_asm<c * 128>(p[c], pa0, X.srd, so); }); };
  auto ld_group = [&](u32x4 (&p)[9], int i) { ld_part(p, i, IC(0), IC(9)); };
  auto xwr = [&](int p) { return ldsb + XBASE + w * 8192 + (8 * p + r8) * 128 + 16 * (s8 ^ (r8 >> 1) ^ (4 * (p & 1))); };
  auto sdst = [&](int i) { return ldsb + SBASE + w * 256 + (8 * ((i + XROT * role) & 3) + r8) * 8; };
  u32 ep0, ep1;
  srd_t srdWS;
  u32 v16, v0, xdst, colswz;
  auto common = [&]() {
    epoch_issue(ep0, ws + tile * WS_TILE + WS_EPOCH);
    epoch_issue(ep1, ws + ((tile + 1) & 15) * WS_TILE + WS_EPOCH);
    srdWS = mksrd(ws, WS_END);
    v16 = l * 16; v0 = l == 0 ? 0u : OOB;
#pragma unroll
    for (int p = 0; p < 4; ++p) X.vx[p] = BIAS + (tok0 + 32 * w + 16 * (p & 1) + 2 * r8 + (p >> 1)) * XROW + 16 * (s8 ^ (r8 >> 1) ^ (4 * (p & 1))) - 1024 * p;
#pragma unroll
    for (int p = 0; p < 4; ++p) pinw(X.vx[p]);
    X.bx = ldsb + XBASE + w * 8192 + tk * 128 + 16 * (g ^ (tk >> 1));
    xdst = ldsb + XBASE + w * 8192;
    colswz = 32 * ((s8 >> 1) ^ (r8 >> 1)) + 16 * (s8 & 1);
  };
  auto ln_fetch = [&]() {
    u32x2 t0, t1;
    asm volatile("ds_read_b64 %0, %2\n\tds_read_b64 %1, %2 offset:128\n\ts_waitcnt lgkmcnt(0)" : "=&v"(t0), "=&v"(t1) : "v"(ldsb + SBASE + w * 256 + tk * 8) : "memory");
    X.rs[0] = __uint_as_float(t0[0]); X.sh[0] = __uint_as_float(t0[1]); X.rs[1] = __uint_as_float(t1[0]); X.sh[1] = __uint_as_float(t1[1]); };
  if (__builtin_expect(role < 3, 1)) {
    u32x4 pA[9], pB[9];
    ld_group(pA, 0); ld_part(pB, 1, IC(0), IC(NFIRST - 9));
    SB0();
    common();
    const u32 slice = sq * 3 + role;
    const u32 om = (sq + role) & 3;
    const u32 gm[3] = {om == 0 ? 1u : om == 1 ? 2u : 0u, om == 0 ? 2u : om == 1 ? 0u : 1u, om == 3 ? 2u : om};
    srd_t dsrd = mksrd_b(w1, DM * W1ROW);
    u32 dvl = (8 * (r8 >> 2) + (r8 & 3)) * W1ROW + colswz;
    u32 dk[6];
#pragma unroll
    for (int c = 0; c < 6; ++c) { const u32 pi = 6 * w + c; dk[c] = BIAS + (16 * (pi & 1) + 4 * ((pi >> 1) & 1)) * W1ROW + slice * 768 + (pi >> 2) * 128 - 1024 * (c & 3); }
    u32 dso = 0, dd1 = 32 * W1ROW, dd2 = 32 * W1ROW, dwrap = ~0u;
    const u32 dm0lo = ldsb + w * 6144, dm0hi = dm0lo + FNS * FSLOT;
    u32 dm0 = dm0lo;
    auto dma_piece = [&](auto C) { constexpr int c = C.value; dma16<(c & 3) * 1024>(dvl, dsrd, dso + dk[c]); };
    auto dma_adv = [&](auto E) {
      if constexpr (E.value == 0) dso += dd1; else { dso += dd2; dso = dso == dwrap ? 0 : dso; }
      dm0 += FSLOT; dm0 = dm0 == dm0hi ? dm0lo : dm0; };
    auto dma_all = [&](auto E) { setm0(dm0); sfor<4>(dma_piece); setm0(dm0 + 4096); dma_piece(IC(4)); dma_piece(IC(5)); dma_adv(E); };
    constexpr int NVA = 20;
    f32x4 acc[24][2];
    auto zacc = [&](auto LO, auto N) { sfor<N.value>([&](auto I) { constexpr int k = LO.value + I.value; if constexpr (k < 2 * NVA) zero_v(acc[k >> 1][k & 1]); else zero_a(acc[k >> 1][k & 1]); }); };
    zacc(IC(0), IC(48));
    u32 cur[4], nxt[4];
    u32 rdelta = FSLOT, rslot = 0;
    constexpr int LA = 8, NF = 12;
    s16x4 Fl[NF], Fh[NF];
    auto rdhalf = [&](auto A, auto T, const u32 (&bs)[4]) { constexpr int a = A.value;
      if constexpr (T.value == 0) FR<a % NF>::template lo<(a >> 2) * 4096>(Fl[a % NF], bs[a & 3]); else FR<a % NF>::template hi<(a >> 2) * 4096 + 2048>(Fh[a % NF], bs[a & 3]); };
#pragma unroll
    for (int u = 0; u < 4; ++u) cur[u] = ldsb + hh * 1024 + 128 * rr + 32 * (u ^ (rr >> 1)) + 8 * p4;
    srd_t psrd = mksrd(w2, DFF * XROW);
    u32 pso_ = (slice * 384 + 48 * t7) * XROW + w * 1024;
    u32 plim_ = (slice * 384 + 48 * t7 + 48) * XROW - 1024;
    ld_part(pB, 1, IC(NFIRST - 9), IC(18 - NFIRST));
    dma_all(IC(0));
    ln_group<15>(pA, sdst(0), xwr(0));
    ld_group(pA, 2);
    ln_group<15>(pB, sdst(1), xwr(1));
    ld_group(pB, 3);
    ln_group<9>(pA, sdst(2), xwr(2));
    dma_all(IC(1));
    __builtin_amdgcn_s_barrier();
    u32x4 B[2], Bn[2], raw[2];
    sfor<LA>([&](auto A) { rdhalf(A, IC(0), cur); rdhalf(A, IC(1), cur); });
    ln_group<6>(pB, sdst(3), xwr(3));
    rd128_v<0>(raw[0], X.bx); rd128_v<2048>(raw[1], X.bx);
    setm0(ldsb + TBASE); dma16<0>(v16, psrd, pso_);
    dma_all(IC(0));
    setm0(ldsb + TBASE); dma16<0>(v16, psrd, pso_);
    STAMP(1);
    ln_fetch();
    epoch_wait(ep0, ep1);
    u32 tl = 0;
    if (TLBW) { tlb_touch<0xF0000>(tl, w1); tlb_touch<0xF0000>(tl, (const char*)w1 + 0x100000); tlb_touch<0xF0000>(tl, (const char*)w1 + 0x200000); tlb_touch<0xF0000>(tl, (const char*)w1 + 0x300000); tlb_touch<0xF0000>(tl, (const char*)w1 + 0x400000);
      tlb_touch<0>(tl, ws + tile * WS_TILE + WS_TILE - 256); tlb_touch<0>(tl, (const char*)out + tok0 * XROW); tlb_touch<127 * XROW>(tl, (const char*)out + tok0 * XROW); }
    pinu(raw[0]); pinu(raw[1]);
    B[0] = norm_frag(raw[0], X.rs[0], X.sh[0]); B[1] = norm_frag(raw[1], X.rs[1], X.sh[1]);
    asm volatile("s_nop 1" : "+v"(B[0]), "+v"(B[1]));
    STAMP(2);
    auto stage = [&](auto E, auto NV, auto DMA, auto L8, auto&& MM, auto&& MEM, auto&& EX, auto&& BAR) {
      sfor<48>([&](auto M) { constexpr int m = M.value, a = m >> 1, tb = m & 1;
        if constexpr (m == 32 && NV.value >= 0) { if constexpr (NV.value < 63) waitvm<NV.value>(); __builtin_amdgcn_s_barrier(); BAR(); }
        if constexpr (m % 8 == 0) wl<(NV.value < 0 && m >= 40) ? 0 : (m == 8 ? L8.value : 8)>();
        MM(M);
        if constexpr (a + LA < 24) rdhalf(IC(a + LA), IC(tb), cur); else if constexpr (NV.value >= 0) rdhalf(IC(a + LA - 24), IC(tb), nxt);
        if constexpr (DMA.value && dpos(m) >= 0) dma_piece(IC(dpos(m)));
        MEM(M);
        SB0();
        if constexpr (DMA.value && m == 2) setm0_add<0>(dm0);
        if constexpr (DMA.value && m == 26) setm0_add<4096>(dm0);
        if constexpr (DMA.value && m == 31) dma_adv(IC(E.value ^ 1));
        if constexpr (m == 1) { rslot += FSLOT; rdelta = rslot == FNS * FSLOT ? 0u - (FNS - 1) * FSLOT : FSLOT; rslot = rslot == FNS * FSLOT ? 0 : rslot; }
        if constexpr (m >= 2 && m < 6 && NV.value >= 0) nxt[m - 2] = v_addv(cur[m - 2], rdelta);
        EX(M);
        SB0();
      });
#pragma unroll
      for (int u = 0; u < 4; ++u) cur[u] = nxt[u];
    };
    auto nop = [&](auto) {};
    auto nop0 = [&]() {};
    constexpr auto tbo = [](int m) { return GRAY ? ((m ^ (m >> 1)) & 1) : (m & 1); };
    u32 xs = 0, xso = 256, ba = X.bx ^ 64;
    u32 tlo[2], thi[2];
    for (int it = 0; it < 9; ++it) {
      sfor<2>([&](auto E) { constexpr int e = E.value;
        if constexpr (e == 0) { if (it == 7) { X.srd = mksrd(x, 0); int last; psrd = code_srd(last); plim_ = (u32)last; pso_ = (t7 & 1) * 16384 + w * 1024; pso_ = pso_ > plim_ ? plim_ : pso_; } }
        if constexpr (e == 1) { if (it == 7) {
            dsrd = mksrd_b(w2, DFF * XROW); dvl = r8 * XROW + colswz;
#pragma unroll
            for (int c = 0; c < 6; ++c) dk[c] = BIAS + (48 * w + 8 * c) * XROW + role * 384 - 1024 * (c & 3);
            dso = slice * 384 * XROW + gm[0] * 128; dd1 = 192 * XROW; dd2 = (gm[1] - gm[0]) * 128 - 192 * XROW;
          } }
        stage(E, IC(e == 0 ? 14 : 18), IC(1), IC(6),
              [&](auto M) { constexpr int a = M.value >> 1, tb = tbo(M.value); if constexpr (a < NVA) FR<a % NF>::vv(acc[a][tb], Fl[a % NF], Fh[a % NF], B[tb]); else FR<a % NF>::av(acc[a][tb], Fl[a % NF], Fh[a % NF], B[tb]); },
              [&](auto M) { constexpr int m = M.value;
                if constexpr (m == 0) rd128_v<0>(raw[0], ba);
                if constexpr (m == 1) rd128_v<2048>(raw[1], ba);
                if constexpr (e == 0 && m >= 34 && m < 46 && (m - 34) % 3 == 0) dma16<((m - 34) / 3) * 1024>(X.vx[(m - 34) / 3], X.srd, xso);
                if constexpr (m == 46) dma16<0>(v16, psrd, pso_); },
              [&](auto M) { constexpr int m = M.value;
                if constexpr (e == 0 && m == 33) setm0_add<0>(xdst + xs);
                if constexpr (e == 0 && m == 44) { xso += 128; xs ^= 4096; }
                if constexpr (m == 45) setm0_add<TBASE>(ldsb);
                if constexpr (m == 47) { pso_ += 4096; pso_ = pso_ > plim_ ? plim_ : pso_; }
                if constexpr (m == 6) ba = v_addv(e == 0 ? X.bx : (X.bx ^ 64), e == 0 ? (xs ^ 4096) : xs);
                if constexpr (m >= 8) norm_op<m - 8>(X, raw, Bn, tlo, thi); },
              nop0);
        B[0] = Bn[0]; B[1] = Bn[1];
        asm volatile("s_nop 0" : "+v"(B[0]), "+v"(B[1]));
      });
    }
    STAMP(3);
    tlb_done(tl);
    asm volatile("s_nop 7");
    u32x4 H[12][2];
    const u32 hb0 = tile * WS_TILE + WS_H + (slice * 4 + w) * 24576;
#pragma unroll
    for (int k = 0; k < 12; ++k)
#pragma unroll
      for (int tb = 0; tb < 2; ++tb) {
        if (2 * k < NVA) { pin(acc[2 * k][tb]); pin(acc[2 * k + 1][tb]); } else { pin_a(acc[2 * k][tb]); pin_a(acc[2 * k + 1][tb]); }
#pragma unroll
        for (int i = 0; i < 4; ++i) { const f32x4 s = acc[2 * k + (i >> 1)][tb];
          H[k][tb][i] = __builtin_bit_cast(u32, __builtin_elementwise_max(__builtin_bit_cast(s16x2, cvtpk(s[2 * (i & 1)], s[2 * (i & 1) + 1])), s16x2{0, 0})); }
        if (k < 3) st128(H[k][tb], srdWS, v16 + ((2 * k + tb) & 3) * 1024, hb0 + (k >> 1) * 4096);
      }
    const u32 tag = (ep0 + 1) << 4;
    const u32 fso = tile * WS_TILE + WS_FLAG + w * 128;
    const u32 slB = sq * 3 + (role == 2 ? 0 : role + 1), slC = sq * 3 + (role == 0 ? 2 : role - 1);
    const u32 sbase[3] = {slice * 384 * XROW, slB * 384 * XROW, slC * 384 * XROW};
    const u32 hbB = tile * WS_TILE + WS_H + (slB * 4 + w) * 24576, hbC = tile * WS_TILE + WS_H + (slC * 4 + w) * 24576;
    const u32 pg0 = tile * WS_TILE + WS_P + (role * 96 + w) * 4096;
    const u32 vpoll = l < 16 ? l * 4 : OOB;
    const u32 lim12 = (l == slB || l == slC) ? 13u : ~0u;
    const bool owner = om < 3;
    const u32 pown = pg0 + gm[2] * 131072;
    const u32 sqi[3] = {(om + 2 >= 3 ? om - 1 : om + 2) - role & 3, (om + 1 >= 3 ? om - 2 : om + 1) - role & 3, (3 - role) & 3};
    const u32 ned[3] = {3, 4, 3 + om};
    bool got[4] = {false, false, false, false}, pend[4] = {false, false, false, false};
    f32x4 ac3[3][4][2], acs[4][2];
    u32x4 HB[12][2], HC[12][2], sd[4], SP[4][4], PQ[3][4];
    u32 pw, pvS = OOB, pv0 = OOB, pv1 = OOB;
    s16x8 sel0, sel1;
    auto rdy = [&](auto I) { constexpr int i = I.value;
      if constexpr (i < 3) return __all(l != sqi[i] * 3 + role || pw - (tag + ned[i]) <= 15u - ned[i]);
      else return __all(l < 12 || l >= 16 || pw - (tag + role * 3 + gm[2] + 1) <= 14u - (role * 3 + gm[2])); };
    auto red_f = [&](u32x4 (&P)[4]) { sfor<4>([&](auto U) { mfma_a(ac3[2][U.value][0], sel0, P[U.value]); mfma_a(ac3[2][U.value][1], sel1, P[U.value]); }); };
    auto red_s = [&]() { sfor<16>([&](auto I) { constexpr int q = I.value >> 2, u = I.value & 3; mfma_a(acs[u][0], sel0, SP[q][u]); mfma_a(acs[u][1], sel1, SP[q][u]); }); };
    auto ld_s = [&](auto I) { constexpr int i = I.value; ld128_a<(i & 3) * 1024>(SP[i >> 2][i & 3], pvS, srdWS, pown + (4 + (i >> 2)) * 16384); };
    const srd_t srdX = mksrd_b(x, SEQ * XROW), srdO = mksrd_b(out, SEQ * XROW);
    const u32 ecol = (role * 3 + gm[2]) * 128;
    asm volatile("s_nop 1");
    STAMP(4);
    auto g2stage = [&](auto OO, auto JJ) { constexpr int J = JJ.value; constexpr bool O = OO.value;
      constexpr auto whf = [](int j) { return j < 6 ? 0 : j < 9 ? 1 : j < 12 ? 2 : 1 + (j & 1); };
      constexpr auto gaf = [](int j) { return j < 6 ? j >> 1 : j < 12 ? j % 3 : (j - 12) >> 1; };
      constexpr auto haf = [](int j) { return j < 6 ? j & 1 : j < 12 ? 0 : 1; };
      constexpr int which = whf(J), gam = gaf(J), half = haf(J), Jn = J + 4;
      constexpr int nw = J <= 16 ? f2count(O, J - 2, 30, J, 32) : -1;
      constexpr int nv = J == 4 ? imin(nw, f2after(O, OP_POLL, 0, J, 32)) : J == 8 ? imin(nw, f2after(O, OP_POLL, 1, J, 32)) : (O && J == 13) ? imin(nw, f2after(O, OP_POLL, 2, J, 32)) : nw;
      stage(IC(J & 1), IC(nv), IC(J <= 14), IC(8),
            [&](auto M) { constexpr int m = M.value, a = m >> 1, tb = tbo(m), u = a & 3, ks = a >> 2, kap = 6 * half + ks;
              if constexpr (m % 8 == 0 && J == 6) waitvm<f2after(O, OP_HB, 2 * ks + 1, J, m)>();
              if constexpr (m == 0 && J == 9) waitvm<f2after(O, OP_HC, 11, J, 0)>();
              if constexpr (m == 0 && J == 12) waitvm<f2after(O, OP_HB, 23, J, 0)>();
              if constexpr (m == 0 && J == 13) waitvm<f2after(O, OP_HC, 23, J, 0)>();
              if constexpr (which == 0) { if constexpr (half == 0 && ks == 0) FR<a % NF>::vv0(ac3[gam][u][tb], Fl[a % NF], Fh[a % NF], H[0][tb]); else FR<a % NF>::vv(ac3[gam][u][tb], Fl[a % NF], Fh[a % NF], H[kap][tb]); }
              else if constexpr (which == 1) FR<a % NF>::va(ac3[gam][u][tb], Fl[a % NF], Fh[a % NF], HB[kap][tb]);
              else FR<a % NF>::va(ac3[gam][u][tb], Fl[a % NF], Fh[a % NF], HC[kap][tb]); },
            [&](auto M) { constexpr int m = M.value, op = f2op(O, J, m), i = f2index(O, J, m);
              if constexpr (op == OP_HS) st128(H[3 + (i >> 1)][i & 1], srdWS, v16 + ((i + 2) & 3) * 1024, hb0 + ((i + 6) >> 2) * 4096);
              if constexpr (op == OP_HB) ld128_a<(i & 3) * 1024>(HB[i >> 1][i & 1], v16, srdWS, hbB + (i >> 2) * 4096);
              if constexpr (op == OP_HC) ld128_a<(i & 3) * 1024>(HC[i >> 1][i & 1], v16, srdWS, hbC + (i >> 2) * 4096);
              if constexpr (op == OP_FS) st128(sd[i & 3], srdWS, v16 + (i & 3) * 1024, pg0 + gm[i >> 2] * 131072 + sq * 16384);
              if constexpr (op == OP_SL) ld_s(IC(i));
              if constexpr (op == OP_PL) { constexpr int q = (i & 4) ? 2 : 0; ld128_a<(i & 3) * 1024>(PQ[q][i & 3], (i & 4) ? pv1 : pv0, srdWS, pown + sqi[q] * 16384); }
              if constexpr (op == OP_MISC) dma16<i * 1024>(X.vx[i], srdX, ecol);
              if constexpr (op == OP_POLL) poll_issue(pw, vpoll, srdWS, fso);
              if constexpr (op == OP_FLAG) {
                waitvm<(i == 0 ? f2after(O, OP_HS, 5, J, m) : i == 1 ? f2after(O, OP_HS, 17, J, m) : f2after(O, OP_FS, 4 * (i - 2) + 3, J, m))>();
                st_flag(tag + 1 + i, srdWS, v0, fso + slice * 4); }
              if constexpr ((J == 14 || J == 16) && m == 13) keep4(sd[0], sd[1], sd[2], sd[3]); },
            [&](auto M) { constexpr int m = M.value;
              if constexpr (m == 40 && Jn < 18) dso = sbase[whf(Jn)] + haf(Jn) * 192 * XROW + gm[gaf(Jn)] * 128;
              if constexpr ((J == 14 || J == 16) && m < 8) { constexpr int u = m >> 1, tb = m & 1, gp = (J - 14) >> 1;
                sd[u][2 * tb] = v_cvtf(ac3[gp][u][tb][0], ac3[gp][u][tb][1]); sd[u][2 * tb + 1] = v_cvtf(ac3[gp][u][tb][2], ac3[gp][u][tb][3]); }
              if constexpr (O && J == 12 && m >= 8 && m < 16) zero_v(acs[(m - 8) >> 1][m & 1]);
              if constexpr (O && J == 12 && m == 20) {
                const u32 on = (tk >> 2) == g ? 0x3f80u << (16 * (tk & 1)) : 0u, s0 = (tk & 2) ? 0u : on, s1 = (tk & 2) ? on : 0u;
                sel0 = __builtin_bit_cast(s16x8, u32x4{s0, s1, 0u, 0u}); sel1 = __builtin_bit_cast(s16x8, u32x4{0u, 0u, s0, s1}); pins(sel0); pins(sel1); }
              if constexpr (O && J == 16 && m == 15) setm0_add<0>(xdst);
              if constexpr (O && J == 16 && m == 44) { waitvm<f2after(O, OP_POLL, 3, J, 45)>(); waitvm_v<63>(pw);
                if (pend[3]) red_s();
                pend[3] = false; pend[0] = owner && rdy(IC(0)); pend[2] = owner && rdy(IC(2)); got[0] = pend[0]; got[2] = pend[2]; pv0 = pend[0] ? v16 : OOB; pv1 = pend[2] ? v16 : OOB; } },
            [&]() {
              if constexpr (J == 4) { waitvm_v<63>(pw); if (!__all(pw - (tag + 1) <= lim12 + (lim12 != ~0u))) spin2(vpoll, srdWS, fso, tag + 1, lim12 + (lim12 != ~0u)); }
              if constexpr (J == 8) { waitvm_v<63>(pw); if (!__all(pw - (tag + 2) <= lim12)) spin2(vpoll, srdWS, fso, tag + 2, lim12); }
              if constexpr (O && J == 13) { waitvm_v<63>(pw); pend[3] = owner && rdy(IC(3)); got[3] = pend[3]; pvS = pend[3] ? v16 : OOB; } });
    };
    sfor<18>([&](auto JJ) { g2stage(std::true_type{}, JJ); if constexpr (JJ.value == 5) STAMP(5); });
    STAMP(6);
    nop8(ac3[2]);
    if (!owner) {
      sfor<8>([&](auto I) { constexpr int u = I.value >> 1, tb = I.value & 1;
        sd[u][2 * tb] = v_cvtf(ac3[2][u][tb][0], ac3[2][u][tb][1]); sd[u][2 * tb + 1] = v_cvtf(ac3[2][u][tb][2], ac3[2][u][tb][3]); });
      asm volatile("s_nop 0");
      sfor<4>([&](auto U) { st128(sd[U.value], srdWS, v16 + U.value * 1024, pown + sq * 16384); });
      waitvm<0>();
      st_flag(tag + 5, srdWS, v0, fso + slice * 4);
      STAMP(7);
      STAMP_FLUSH();
      return;
    }
    sfor<4>([&](auto U) { undef_a(PQ[1][U.value]); });
#pragma unroll 1
    for (u32 it = 0; it < SPIN_MAX; ++it) {
      waitvm_v<0>(pw);
      if (pend[3]) red_s();
      if (got[0] && got[1] && got[2] && got[3]) break;
      sfor<3>([&](auto I) { constexpr int i = I.value; pend[i] = !got[i] && rdy(I);
        if (pend[i]) { got[i] = true; sfor<4>([&](auto U) { ld128_a<U.value * 1024>(PQ[i][U.value], v16, srdWS, pown + sqi[i] * 16384); }); } });
      pend[3] = !got[3] && rdy(IC(3));
      if (pend[3]) { got[3] = true; pvS = v16; sfor<16>(ld_s); }
      if (!(got[0] && got[1] && got[2] && got[3])) poll_issue(pw, vpoll, srdWS, fso);
    }
    waitvm<0>();
    nop8(ac3[2]);
    red_f(PQ[0]); red_f(PQ[1]); red_f(PQ[2]);
    asm volatile("s_nop 7");
    const u32 ia0 = xdst + tk * 128 + 16 * (hh ^ (tk >> 1)) + 8 * (g & 1);
#pragma unroll
    for (int u = 0; u < 4; ++u)
#pragma unroll
      for (int tb = 0; tb < 2; ++tb) {
        pin(ac3[2][u][tb]); pin(acs[u][tb]);
        const u32 ia = (ia0 ^ (32 * u)) + tb * 2048;
        const u32x2 xv = rd64(ia);
        u32x2 o;
#pragma unroll
        for (int d = 0; d < 2; ++d) {
          const f32x2 f = {ac3[2][u][tb][2 * d], ac3[2][u][tb][2 * d + 1]}, sv = {acs[u][tb][2 * d], acs[u][tb][2 * d + 1]};
          const f32x2 t = f * -1.4426950408889634f;
          const f32x2 e = f32x2{__builtin_amdgcn_exp2f(t[0]), __builtin_amdgcn_exp2f(t[1])} + 1.f;
          const f32x2 gt = f * f32x2{__builtin_amdgcn_rcpf(e[0]), __builtin_amdgcn_rcpf(e[1])};
          const f32x2 r = __builtin_elementwise_fma(sv, gt, f32x2{bflo(xv[d]), bfhi(xv[d])});
          o[d] = cvtpk(r[0], r[1]);
        }
        wr64(ia, o);
      }
#pragma unroll
    for (int c = 0; c < 4; ++c) __builtin_amdgcn_raw_buffer_store_b128(rd128(xdst + c * 1024 + v16), srdO, X.vx[c] + c * 1024, ecol, 0);
    if (role == 0 && om == 0 && w == 0) st_flag(ep0 + 1, srdWS, v0, tile * WS_TILE + WS_EPOCH);
    STAMP(7);
    STAMP_FLUSH();
  } else {
    u32x4 pA[9], pB[9];
    ld_group(pA, 0); ld_part(pB, 1, IC(0), IC(NFIRST - 9));
    SB0();
    common();
    srd_t dsrd = mksrd_b(wqkv, DM * WQROW);
    u32 dv[7];
#pragma unroll
    for (int c = 0; c < 7; ++c) { u32 pi = 7 * w + c; pi = pi > 26 ? 26 : pi;
      const u32 seg = pi / 9, idx = (pi - seg * 9) * 64 + l, rho = idx / 18, ch = idx - rho * 18;
      const u32 krow = 16 * ((rho >> 3) & 1) + 4 * (rho >> 4) + 8 * ((rho >> 2) & 1) + (rho & 3);
      dv[c] = BIAS + krow * WQROW + seg * 1152 + sq * 288 + ch * 16 - 1024 * (c & 3); }
    u32 dso = 0;
    const u32 dm0lo = ldsb + w * 7168, dm0hi = dm0lo + ANS * ASLOT;
    u32 dm0 = dm0lo;
    auto dma_piece = [&](auto C) { constexpr int c = C.value; dma16<(c & 3) * 1024>(dv[c], dsrd, dso); };
    auto dma_adv = [&]() { dso += 32 * WQROW; dm0 += ASLOT; dm0 = dm0 == dm0hi ? dm0lo : dm0; };
    auto dma_all = [&]() { setm0(dm0); sfor<4>(dma_piece); setm0(dm0 + 4096); dma_piece(IC(4)); dma_piece(IC(5)); dma_piece(IC(6)); dma_adv(); };
    f32x4 acc[27][2];
    auto zacc = [&](auto LO, auto N) { sfor<N.value>([&](auto I) { constexpr int k = LO.value + I.value; if constexpr (k < 18) zero_a(acc[k >> 1][k & 1]); else zero_v(acc[k >> 1][k & 1]); }); };
    zacc(IC(0), IC(54));
    u32 cur = ldsb + (8 * hh + rr) * 288 + 8 * p4, nxt = cur, rslot = 0, rdelta = ASLOT;
    constexpr int LA = 6;
    s16x4 Fl[9], Fh[9];
    auto rdhalf = [&](auto A, auto T, u32 bs) { constexpr int a = A.value, off = (a / 9) * 9216 + (a % 9) * 32;
      if constexpr (T.value == 0) FR<a % 9>::template lo<off>(Fl[a % 9], bs); else FR<a % 9>::template hi<off + 16 * 288>(Fh[a % 9], bs); };
    const srd_t psrd = mksrd(wqkv, DM * WQROW);
    const u32 pf0 = l < 48 ? (4 * t7 + l / 12) * WQROW + ((sq * 288) & ~127u) + (l % 12) * 32 : OOB;
    u32 pfl = w < 3 ? pf0 + w * 1152 : l * 32;
    srd_t psrdl = w < 3 ? psrd : mksrd(wo, DM * XROW);
    const int plo_ = w < 3 ? 0 : (144 * sq + 18 * t7) * XROW;
    int phi_ = w < 3 ? 17 * 32 * WQROW : plo_ + 18 * XROW - 2048, pst_ = w < 3 ? 32 * WQROW : 2048;
    int pso_ = w < 3 ? 6 * 32 * WQROW : plo_ - 7 * 2048;
    ld_part(pB, 1, IC(NFIRST - 9), IC(18 - NFIRST));
    dma_all();
    ln_group<16>(pA, sdst(0), xwr(0));
    ld_group(pA, 2);
    ln_group<16>(pB, sdst(1), xwr(1));
    ld_group(pB, 3);
    ln_group<9>(pA, sdst(2), xwr(2));
    dma_all();
    __builtin_amdgcn_s_barrier();
    u32x4 B[2], Bn[2], raw[2];
    sfor<LA>([&](auto A) { rdhalf(A, IC(0), cur); rdhalf(A, IC(1), cur); });
    ln_group<7>(pB, sdst(3), xwr(3));
    rd128_v<0>(raw[0], X.bx); rd128_v<2048>(raw[1], X.bx);
    setm0(ldsb + TBASE); sfor<3>([&](auto S) { touch(pf0, psrd, (2 + w) * 32 * WQROW + S.value * 1152); });
    STAMP(1);
    ln_fetch();
    epoch_wait(ep0, ep1);
    u32 tl = 0;
    if (TLBW) { tlb_touch<0xF0000>(tl, wqkv); tlb_touch<0xE0000>(tl, (const char*)wqkv + 0x100000); tlb_touch<0>(tl, wo); tlb_touch<0xA0000>(tl, wo); }
    pinu(raw[0]); pinu(raw[1]);
    B[0] = norm_frag(raw[0], X.rs[0], X.sh[0]); B[1] = norm_frag(raw[1], X.rs[1], X.sh[1]);
    asm volatile("s_nop 1" : "+v"(B[0]), "+v"(B[1]));
    STAMP(2);
    u32 xs = 0, xso = 256, ba = X.bx ^ 64;
    u32 tlo[2], thi[2];
    for (int it = 0; it < 9; ++it) {
      if (it == 6 && w < 3) { psrdl = code_srd(phi_); pfl = l * 32; pso_ = (int)w * 2048 > phi_ ? phi_ : (int)w * 2048; pst_ = 6144; }
      if (it == 7) X.srd = mksrd(x, 0);
      if (it == 8) dsrd = mksrd(wqkv, 0);
      sfor<2>([&](auto E) { constexpr int e = E.value;
        sfor<54>([&](auto M) { constexpr int m = M.value, a = m >> 1, tb = m & 1, tq = GRAY ? ((m ^ (m >> 1)) & 1) : tb;
          if constexpr (m == 42) { waitvm<e == 0 ? 8 : 12>(); __builtin_amdgcn_s_barrier(); }
          if constexpr (m % 6 == 0) wl<6>();
          if constexpr (a < 9) FR<a % 9>::av(acc[a][tq], Fl[a % 9], Fh[a % 9], B[tq]); else FR<a % 9>::vv(acc[a][tq], Fl[a % 9], Fh[a % 9], B[tq]);
          if constexpr (a + LA < 27) rdhalf(IC(a + LA), IC(tb), cur); else rdhalf(IC(a + LA - 27), IC(tb), nxt);
          if constexpr (m % 6 == 3 && m < 42) dma_piece(IC(m / 6));
          if constexpr (m == 0) rd128_v<0>(raw[0], ba);
          if constexpr (m == 1) rd128_v<2048>(raw[1], ba);
          if constexpr (e == 0 && m >= 44 && m < 52 && (m & 1) == 0) dma16<((m - 44) / 2) * 1024>(X.vx[(m - 44) / 2], X.srd, xso);
          if constexpr (m == 52) touch(pfl, psrdl, (u32)(pso_ < plo_ ? plo_ : pso_));
          SB0();
          if constexpr (m == 2) setm0_add<0>(dm0);
          if constexpr (m == 26) setm0_add<4096>(dm0);
          if constexpr (m == 40) dma_adv();
          if constexpr (e == 0 && m == 43) setm0_add<0>(xdst + xs);
          if constexpr (e == 0 && m == 53) { xso += 128; xs ^= 4096; }
          if constexpr (m == 51) setm0_add<TBASE>(ldsb);
          if constexpr (m == 53) { pso_ += pst_; pso_ = pso_ > phi_ ? phi_ : pso_; }
          if constexpr (m == 1) { rslot += ASLOT; rdelta = rslot == ANS * ASLOT ? 0u - (ANS - 1) * ASLOT : ASLOT; rslot = rslot == ANS * ASLOT ? 0 : rslot; }
          if constexpr (m == 4) nxt = v_addv(cur, rdelta);
          if constexpr (m == 6) ba = v_addv(e == 0 ? X.bx : (X.bx ^ 64), e == 0 ? (xs ^ 4096) : xs);
          if constexpr (m >= 12 && m < 52) norm_op<m - 12>(X, raw, Bn, tlo, thi);
          SB0();
        });
        cur = nxt; B[0] = Bn[0]; B[1] = Bn[1];
      });
    }
    STAMP(3);
    tlb_done(tl);
    asm volatile("s_nop 7\n\ts_waitcnt vmcnt(0) lgkmcnt(0)" ::: "memory");
    u32x4 Kp[9], Kq[9], Vp[9];
    u32x2 Qp[9][2];
    const bool hsend = w == 3 && tile < 15;
    const u32 hsn = (tile + 1) * WS_TILE + WS_HALO + sq * 18432, hfn = (tile + 1) * WS_TILE + WS_HFLAG + sq * 128;
#pragma unroll
    for (int b = 0; b < 9; ++b) { pin(acc[9 + b][0]); pin(acc[9 + b][1]);
      Kp[b] = u32x4{cvtpk(acc[9 + b][0][0], acc[9 + b][0][1]), cvtpk(acc[9 + b][0][2], acc[9 + b][0][3]), cvtpk(acc[9 + b][1][0], acc[9 + b][1][1]), cvtpk(acc[9 + b][1][2], acc[9 + b][1][3])}; }
    if (hsend) {
#pragma unroll
      for (int b = 0; b < 9; ++b) st128(Kp[b], srdWS, v16, hsn + b * 1024);
    }
#pragma unroll
    for (int b = 0; b < 9; ++b) { pin(acc[18 + b][0]); pin(acc[18 + b][1]);
      Vp[b] = u32x4{cvtpk(acc[18 + b][0][0], acc[18 + b][0][1]), cvtpk(acc[18 + b][0][2], acc[18 + b][0][3]), cvtpk(acc[18 + b][1][0], acc[18 + b][1][1]), cvtpk(acc[18 + b][1][2], acc[18 + b][1][3])}; }
    __builtin_amdgcn_s_barrier();
    const srd_t wsrd = mksrd_b(wo, DM * XROW);
    const u32 wvl = r8 * XROW + colswz;
    constexpr auto wslot = [](int G) { return G < 3 ? 57344 + G * 20480 : G < 5 ? (G - 3) * 20480 : G == 5 ? (int)XBASE : 57344 + (G - 6) * 20480; };
    u32 wk[5];
#pragma unroll
    for (int c = 0; c < 5; ++c) { u32 pi = 5 * w + c; pi = pi > 17 ? 17 : pi; wk[c] = BIAS + (144 * sq + 8 * pi) * XROW - 1024 * (c & 3); }
    const u32 wm0 = ldsb + w * 5120;
    auto wo_piece = [&](auto GG, auto C) { constexpr int G = GG.value, c = C.value;
      if constexpr ((c & 3) == 0) { setm0_add<wslot(G) + c * 1024>(wm0); asm volatile("s_nop 0"); }
      dma16<(c & 3) * 1024>(wvl, wsrd, wk[c] + G * 128); };
    auto wo_dma = [&](auto GG) { sfor<5>([&](auto C) { wo_piece(GG, C); }); };
    const u32 vw = ldsb + (16 * (w + 1) + tk) * VROW + 8 * g;
    auto wr_v = [&]() {
#pragma unroll
      for (int b = 0; b < 9; ++b) { wr64(vw + 32 * b, u32x2{Vp[b][0], Vp[b][1]}); wr64(vw + 80 * VROW + 32 * b, u32x2{Vp[b][2], Vp[b][3]}); } };
    if (w < 3) {
      wr_v();
#pragma unroll
      for (int b = 0; b < 9; ++b) wr128(ldsb + XBASE + w * 9216 + b * 1024 + v16, Kp[b]);
      waitlgkm<0>();
    } else if (hsend) {
      waitvm<0>(); st_flag(((ep1 + 1) << 4) + 1, srdWS, v0, hfn);
#pragma unroll
      for (int b = 0; b < 9; ++b) st128(Vp[b], srdWS, v16, hsn + 9216 + b * 1024);
    }
    __builtin_amdgcn_s_barrier();
    if (w == 3) wr_v();
#pragma unroll
    for (int b = 0; b < 9; ++b)
#pragma unroll
      for (int tb = 0; tb < 2; ++tb) { pin_a(acc[b][tb]); Qp[b][tb] = u32x2{cvtpk(acc[b][tb][0], acc[b][tb][1]), cvtpk(acc[b][tb][2], acc[b][tb][3])}; }
#pragma unroll
    for (int b = 0; b < 9; ++b) pinu2(Qp[b][0], Qp[b][1]);
    u32x4 Vh[9];
    const u32 hv = tile > 0 ? v16 : OOB;
    const u32 hso = tile * WS_TILE + WS_HALO + sq * 18432, hfo = tile * WS_TILE + WS_HFLAG + sq * 128;
    if (hsend) { waitvm<0>(); st_flag(((ep1 + 1) << 4) + 2, srdWS, v0, hfn); }
    if (w > 0) {
#pragma unroll
      for (int b = 0; b < 9; ++b) Kq[b] = rd128(ldsb + XBASE + (w - 1) * 9216 + b * 1024 + v16);
    } else {
      if (tile > 0) spin(v0, srdWS, hfo, (ep0 + 1) << 4, l == 0 ? 1u : 0u);
#pragma unroll
      for (int b = 0; b < 9; ++b) ld128_vs(Kq[b], hv, srdWS, hso + b * 1024);
    }
    wo_dma(IC(0)); wo_dma(IC(1)); wo_dma(IC(2));
    wait9<15>(Kq);
    STAMP(4);
    f32x4 cmc, cmp; u32 mcur[2];
    {
      const bool hasprev = tile > 0 || w > 0;
      u32 mb[4];
#pragma unroll
      for (int r = 0; r < 4; ++r) { const int i = 4 * g + r, q = tk; const bool vc = i <= q && i + 7 >= q, vp = hasprev && i >= q + 9;
        cmc[r] = vc ? 0.f : -1e30f; cmp[r] = vp ? 0.f : -1e30f; mb[r] = vc ? 0xffffu : 0u; }
      mcur[0] = mb[0] | (mb[1] << 16); mcur[1] = mb[2] | (mb[3] << 16);
    }
    s16x8 ones = {0x3f80, 0x3f80, 0x3f80, 0x3f80, 0x3f80, 0x3f80, 0x3f80, 0x3f80};
    const float sc = 0.23570226039551584f * 1.4426950408889634f;
    const u32 vr = ldsb + (16 * w + 4 * g + q4) * VROW + 8 * p4;
    f32x4 Sc[2][8], Sp[2][8], Ol[2][8], Oh[2][8], Sm[2][8];
    u32x4 Pf[2][8];
    s16x8 Vt[2][9];
    u32x2 qh[8], ql[8], Ob[2][9];
    auto qmask = [&](auto TB) { constexpr int tb = TB.value;
#pragma unroll
      for (int h = 0; h < 8; ++h) {
#pragma unroll
        for (int d = 0; d < 2; ++d) { qh[h][d] = (2 * g + d >= (u32)h) ? Qp[h][tb][d] : 0u; ql[h][d] = (2 * g + d <= (u32)h) ? Qp[h + 1][tb][d] : 0u; }
        pinu2(qh[h], ql[h]); }
      asm volatile("s_nop 1"); };
    auto phA = [&](auto TB, auto HP, auto J) { constexpr int tb = TB.value, j = J.value, h = 2 * HP.value + ((j >> 1) & 1);
      if constexpr (j < 4) { if constexpr (j & 1) mfma16_c(Sp[tb][h], u32x2{Kq[h][2 * tb], Kq[h][2 * tb + 1]}, qh[h], cmp); else mfma16_c(Sc[tb][h], u32x2{Kp[h][2 * tb], Kp[h][2 * tb + 1]}, qh[h], cmc); }
      else { if constexpr (j & 1) mfma16_v(Sp[tb][h], u32x2{Kq[h + 1][2 * tb], Kq[h + 1][2 * tb + 1]}, ql[h]); else mfma16_v(Sc[tb][h], u32x2{Kp[h + 1][2 * tb], Kp[h + 1][2 * tb + 1]}, ql[h]); } };
    f32x4 val[2]; float mx[2];
    auto phB = [&](auto TB, auto HP, auto I) { constexpr int tb = TB.value, i = I.value;
#pragma unroll
      for (int e = 0; e < 2; ++e) { const int h = 2 * HP.value + e;
        if constexpr (i == 0) { pin(Sc[tb][h]); pin(Sp[tb][h]);
#pragma unroll
          for (int r = 0; r < 4; ++r) val[e][r] = fmax2(Sc[tb][h][r], Sp[tb][h][r]);
          mx[e] = fmax2(fmax2(val[e][0], val[e][1]), fmax2(val[e][2], val[e][3])); }
        if constexpr (i == 1) {
          { auto s = __builtin_amdgcn_permlane16_swap(__float_as_uint(mx[e]), __float_as_uint(mx[e]), false, false); mx[e] = fmax2(__uint_as_float(s[0]), __uint_as_float(s[1])); }
          { auto s = __builtin_amdgcn_permlane32_swap(__float_as_uint(mx[e]), __float_as_uint(mx[e]), false, false); mx[e] = fmax2(__uint_as_float(s[0]), __uint_as_float(s[1])); }
          mx[e] = -mx[e] * sc; }
        if constexpr (i == 2) {
#pragma unroll
          for (int r = 0; r < 4; ++r) val[e][r] = __builtin_amdgcn_exp2f(__builtin_fmaf(val[e][r], sc, mx[e])); }
        if constexpr (i == 3) { const u32 k0 = cvtpk(val[e][0], val[e][1]), k1 = cvtpk(val[e][2], val[e][3]);
          Pf[tb][h] = u32x4{k0 & ~mcur[0], k1 & ~mcur[1], k0 & mcur[0], k1 & mcur[1]}; } }
      if constexpr (i < 2) pin2(mx[0], mx[1]);
      if constexpr (i == 2) { pin(val[0]); pin(val[1]); }
      if constexpr (i == 3) { pinu(Pf[tb][2 * HP.value]); pinu(Pf[tb][2 * HP.value + 1]); } };
    auto phC = [&](auto TB, auto HP, auto J) { constexpr int tb = TB.value, j = J.value, h = 2 * HP.value + j / 3, i = j % 3;
      if constexpr (j < 6) {
        if constexpr (i == 0) mfma_v0(Sm[tb][h], ones, Pf[tb][h]);
        if constexpr (i == 1) mfma_v0(Ol[tb][h], Vt[tb][h], Pf[tb][h]);
        if constexpr (i == 2) mfma_v0(Oh[tb][h], Vt[tb][h + 1], Pf[tb][h]); } };
    auto phD = [&](auto TB, auto HD) { constexpr int tb = TB.value, h = HD.value;
      pin(Sm[tb][h]); pin(Ol[tb][h]); pin(Oh[tb][h]);
      const float inv = __builtin_amdgcn_rcpf(Sm[tb][h][0]);
#pragma unroll
      for (int d = 0; d < 2; ++d) { const u32 lo = cvtpk(Ol[tb][h][2 * d] * inv, Ol[tb][h][2 * d + 1] * inv);
        if (h == 0) Ob[tb][0][d] = lo; else Ob[tb][h][d] = (2 * g + d < (u32)h) ? Ob[tb][h][d] : lo;
        Ob[tb][h + 1][d] = cvtpk(Oh[tb][h][2 * d] * inv, Oh[tb][h][2 * d + 1] * inv); }
      pinu2(Ob[tb][h], Ob[tb][h + 1]); };
    auto ld_vt = [&](auto TB) { constexpr int tb = TB.value;
      asm volatile("" ::: "memory");
#pragma unroll
      for (int b = 0; b < 9; ++b) Vt[tb][b] = cat(trrd(vr + tb * 80 * VROW + 32 * b), trrd(vr + (tb * 80 + 16) * VROW + 32 * b)); };
    asm volatile("" : "+v"(cmc), "+v"(cmp), "+v"(ones));
    qmask(IC(0));
    sfor<32>([&](auto J) { phA(IC(0), IC(J.value >> 3), IC(J.value & 7)); });
    qmask(IC(1));
#pragma unroll
    for (int b = 0; b < 9; ++b) undef_a(Vh[b]);
    if (w == 0) {
      if (tile > 0) spin(v0, srdWS, hfo, (ep0 + 1) << 4, l == 0 ? 2u : 0u);
#pragma unroll
      for (int b = 0; b < 9; ++b) ld128_a<0>(Vh[b], hv, srdWS, hso + 9216 + b * 1024);
    }
    asm volatile("s_nop 7");
    sfor<16>([&](auto J) { constexpr int hp = J.value >> 2, i = J.value & 3; phA(IC(1), IC(hp), IC(2 * i)); phA(IC(1), IC(hp), IC(2 * i + 1)); phB(IC(0), IC(hp), IC(i)); });
    if (w == 0) {
      waitvm<0>();
#pragma unroll
      for (int b = 0; b < 9; ++b) { wr64_a(vw - 16 * VROW + 32 * b, Vh[b]); }
    }
    ld_vt(IC(0));
    asm volatile("s_nop 7" :: "v"(cmc), "v"(cmp));
    sfor<16>([&](auto J) { constexpr int hp = J.value >> 2, i = J.value & 3; phC(IC(0), IC(hp), IC(2 * i)); phC(IC(0), IC(hp), IC(2 * i + 1)); phB(IC(1), IC(hp), IC(i)); });
    ld_vt(IC(1));
    asm volatile("s_nop 7");
    sfor<8>([&](auto J) { constexpr int h = J.value; phC(IC(1), IC(h >> 1), IC(3 * (h & 1))); phC(IC(1), IC(h >> 1), IC(3 * (h & 1) + 1)); phC(IC(1), IC(h >> 1), IC(3 * (h & 1) + 2)); phD(IC(0), J); });
    asm volatile("s_nop 7");
    sfor<8>([&](auto J) { phD(IC(1), J); });
    STAMP(5);
    asm volatile("s_waitcnt vmcnt(0) lgkmcnt(0)" ::: "memory");
    __builtin_amdgcn_s_barrier();
    u32x4 OB[4][2];
#pragma unroll
    for (int j = 0; j < 4; ++j)
#pragma unroll
      for (int tb = 0; tb < 2; ++tb) { OB[j][tb] = u32x4{Ob[tb][2 * j][0], Ob[tb][2 * j][1], Ob[tb][2 * j + 1][0], Ob[tb][2 * j + 1][1]}; pinu(OB[j][tb]); }
    pinu2(Ob[0][8], Ob[1][8]);
    u32 wb[3][4];
#pragma unroll
    for (int u = 0; u < 4; ++u) { wb[0][u] = ldsb + hh * 1024 + 128 * rr + 32 * (u ^ (rr >> 1)) + 8 * p4; wb[1][u] = wb[0][u] + 57344; wb[2][u] = wb[0][u] + XBASE; }
    const u32 tag = (ep0 + 1) << 4, fso = tile * WS_TILE + WS_FLAG + w * 128 + (12 + sq) * 4;
    const u32 sbase = tile * WS_TILE + WS_P + ((4 + sq) * 4 + w) * 4096;
    f32x4 as[4][2];
    u32x4 sd[4];
    s16x4 Wl[12], Wh[12];
    auto wrd = [&](auto FI, auto T) { constexpr int f = FI.value, G = f / 20, k = (f % 20) >> 2, u = f & 3;
      if constexpr (G < 9) { constexpr int so = wslot(G), bs = so >= (int)XBASE ? 2 : so >= 57344 ? 1 : 0, rel = so - (bs == 2 ? (int)XBASE : bs == 1 ? 57344 : 0);
        if constexpr (T.value == 0) FR<f % 12>::template lo<rel + k * 4096>(Wl[f % 12], wb[bs][u]);
        else if constexpr (k < 4) FR<f % 12>::template hi<rel + k * 4096 + 2048>(Wh[f % 12], wb[bs][u]); } };
    auto cvt_s = [&](auto I) { constexpr int u = I.value >> 1, tb = I.value & 1; sd[u][2 * tb] = v_cvtf(as[u][tb][0], as[u][tb][1]); sd[u][2 * tb + 1] = v_cvtf(as[u][tb][2], as[u][tb][3]); };
    sfor<8>([&](auto F) { wrd(F, IC(0)); wrd(F, IC(1)); });
    sfor<9>([&](auto GG) { constexpr int G = GG.value;
      if constexpr (G >= 1) cvt_s(IC(0));
      sfor<40>([&](auto J) { constexpr int j = J.value, k = j >> 3, u = (j >> 1) & 3, tb = j & 1, f = 20 * G + (j >> 1);
        if constexpr (j == 30 && G >= 4 && G != 5) { waitvm<4>(); st_flag(tag + G - 1, srdWS, v0, fso); }
        if constexpr (j == 24 && G == 2) waitvm<16>();
        if constexpr (j == 24 && G == 5) { waitvm<3>(); st_flag(tag + 4, srdWS, v0, fso); }
        if constexpr ((j == 0 && G >= 1 && G <= 3) || (j == 24 && (G == 2 || G == 5))) __builtin_amdgcn_s_barrier();
        if constexpr (j % 8 == 0) wl<(G == 8 && k == 4) ? 0 : (k == 3 ? 4 : 8)>();
        if constexpr (k == 0) FR<f % 12>::vv0(as[u][tb], Wl[f % 12], Wh[f % 12], OB[0][tb]);
        else if constexpr (k < 4) FR<f % 12>::vv(as[u][tb], Wl[f % 12], Wh[f % 12], OB[k][tb]);
        else FR<f % 12>::k16(as[u][tb], Wl[f % 12], Ob[tb][8]);
        wrd(IC(f + 8), IC(tb));
        if constexpr (G == 0 && j < 30 && (j & 1)) wo_piece(IC(3 + j / 10), IC((j % 10) >> 1));
        if constexpr (G >= 1 && G <= 3 && j % 6 == 3 && j < 30) wo_piece(IC(G + 5), IC(j / 6));
        if constexpr (G >= 1 && j >= 6 && j <= 24 && j % 6 == 0) st128(sd[j / 6 - 1], srdWS, v16 + (j / 6 - 1) * 1024, sbase + (G - 1) * 131072);
        if constexpr (G >= 1 && j == 26) keep4(sd[0], sd[1], sd[2], sd[3]);
        SB0();
        if constexpr (G >= 1 && j + 1 < 8) cvt_s(IC(j + 1));
        SB0();
      });
    });
    asm volatile("s_nop 7");
    sfor<8>(cvt_s);
    asm volatile("s_nop 0");
    sfor<4>([&](auto U) { st128(sd[U.value], srdWS, v16 + U.value * 1024, sbase + 8 * 131072); });
    waitvm<0>();
    st_flag(tag + 9, srdWS, v0, fso);
    STAMP(6); STAMP(7);
    STAMP_FLUSH();
    asm volatile("fused_code_end:");
  }
}

extern "C" void kernel_launch(void* const* d_in, const int* in_sizes, int n_in, void* d_out, int out_size, void* d_ws, size_t ws_size, hipStream_t stream) {
  (void)hipFuncSetAttribute((const void*)fused, hipFuncAttributeMaxDynamicSharedMemorySize, LDS_BYTES);
  fused<<<256, 256, LDS_BYTES, stream>>>((const u16*)d_in[0], (const u16*)d_in[1], (const u16*)d_in[2], (const u16*)d_in[4], (const u16*)d_in[5], (u16*)d_out, (char*)d_ws);
}
```

```cpp
#include <hip/hip_runtime.h>
#include <utility>
#include <type_traits>
#define XROT 2
#define ABL 0
#define NFIRST 15
#define PAUX "sc1"
#define TLBW 1
#define SAUX 16
#define GRAY 0
typedef unsigned short u16;
typedef unsigned u32;
typedef short s16x2 __attribute__((ext_vector_type(2)));
typedef short s16x4 __attribute__((ext_vector_type(4)));
typedef short s16x8 __attribute__((ext_vector_type(8)));
typedef __bf16 bf16x8 __attribute__((ext_vector_type(8)));
typedef __bf16 bf16x2 __attribute__((ext_vector_type(2)));
typedef float f32x2 __attribute__((ext_vector_type(2)));
typedef float f32x4 __attribute__((ext_vector_type(4)));
typedef u32 u32x2 __attribute__((ext_vector_type(2)));
typedef u32 u32x4 __attribute__((ext_vector_type(4)));
#define LDS3 __attribute__((address_space(3)))
#define DI __device__ __forceinline__
#define SB0() __builtin_amdgcn_sched_barrier(0)
#define IC(x) std::integral_constant<int, (x)>{}
template <int... Is, class F> DI void sfor_impl(std::integer_sequence<int, Is...>, F&& f) { (f(std::integral_constant<int, Is>{}), ...); }
template <int N, class F> DI void sfor(F&& f) { sfor_impl(std::make_integer_sequence<int, N>{}, f); }
typedef __amdgpu_buffer_rsrc_t srd_t;

constexpr int SEQ = 2048, DM = 576, DFF = 4608;
constexpr u32 XROW = 1152, W1ROW = 9216, WQROW = 3456, BIAS = 4096, OOB = 0x80000000u;
constexpr u32 WS_TILE = 4u << 20;
constexpr u32 WS_EPOCH = 0;
constexpr u32 WS_FLAG = 256;
constexpr u32 WS_HFLAG = 1024;
constexpr u32 WS_HALO = 4096;
constexpr u32 WS_P = 128u << 10;
constexpr u32 WS_O = 768u << 10;
constexpr u32 WS_H = 2u << 20;
constexpr u32 WS_END = 16 * WS_TILE;
constexpr u32 WS_STAMP = 64u << 20;
constexpr u32 FSLOT = 24576, FNS = 5, ASLOT = 28672, ANS = 4, XBASE = 122880, TBASE = 155648, SBASE = 156672, LDS_BYTES = 157696;
constexpr u32 VROW = 296;
constexpr u32 SPIN_MAX = 1u << 16;

DI srd_t mksrd(const void* p, u32 n) { return __builtin_amdgcn_make_buffer_rsrc((void*)p, (short)0, (int)n, 0x00020000); }
DI srd_t mksrd_b(const void* p, u32 n) { return mksrd((const char*)p - BIAS, n + BIAS); }
DI void setm0(u32 v) { asm volatile("s_mov_b32 m0, %0\n\ts_nop 0" :: "s"(v) : "memory"); }
template <int B> DI void setm0_add(u32 a) { asm volatile("s_add_u32 m0, %0, %1" :: "s"(a), "i"(B) : "memory", "scc"); }
template <int OFF> DI void dma16(u32 voff, srd_t srd, u32 soff) {
  asm volatile("buffer_load_dwordx4 %0, %1, %2 offen offset:%3 lds" :: "v"(voff), "s"(srd), "s"(soff), "i"(OFF) : "memory");
}
template <int OFF> DI void ld128_asm(u32x4& d, u32 voff, srd_t srd, u32 soff) {
  asm volatile("buffer_load_dwordx4 %0, %1, %2, %3 offen offset:%4" : "=v"(d) : "v"(voff), "s"(srd), "s"(soff), "i"(OFF) : "memory");
}
DI void touch(u32 voff, srd_t srd, u32 soff) { if (ABL & 1) voff = OOB; asm volatile("buffer_load_dword %0, %1, %2 offen lds" :: "v"(voff), "s"(srd), "s"(soff) : "memory"); }
DI void poll_issue(u32& d, u32 voff, srd_t srd, u32 soff) { asm volatile("buffer_load_dword %0, %1, %2, %3 offen sc1" : "=v"(d) : "v"(voff), "s"(srd), "s"(soff) : "memory"); }
template <int N> DI void waitvm() { asm volatile("s_waitcnt vmcnt(%0)" :: "i"(N) : "memory"); }
template <int N> DI void waitvm_v(u32& d) { asm volatile("s_waitcnt vmcnt(%1)" : "+v"(d) : "i"(N) : "memory"); }
template <int N> DI void wait9(u32x4 (&p)[9]) {
  asm volatile("s_waitcnt vmcnt(%9)" : "+v"(p[0]), "+v"(p[1]), "+v"(p[2]), "+v"(p[3]), "+v"(p[4]), "+v"(p[5]), "+v"(p[6]), "+v"(p[7]), "+v"(p[8]) : "i"(N) : "memory");
}
template <int N> DI void waitlgkm() { __builtin_amdgcn_s_waitcnt(0xC07F | (N << 8)); }
DI s16x4 trrd(u32 a) { return __builtin_amdgcn_ds_read_tr16_b64_v4i16((LDS3 s16x4*)(size_t)a); }
DI u32x4 rd128(u32 a) { return *(LDS3 u32x4*)(size_t)a; }
DI void wr128(u32 a, u32x4 v) { *(LDS3 u32x4*)(size_t)a = v; }
DI void wr64(u32 a, u32x2 v) { *(LDS3 u32x2*)(size_t)a = v; }
DI u32 cvtpk(float lo, float hi) { f32x2 v = {lo, hi}; return __builtin_bit_cast(u32, __builtin_convertvector(v, bf16x2)); }
DI float bflo(u32 d) { return __uint_as_float(d << 16); }
DI float bfhi(u32 d) { return __uint_as_float(d & 0xffff0000u); }
DI float rbf(float f) { return bflo(cvtpk(f, 0.f)); }
DI float dot2(u32 a, u32 b, float c) { return __builtin_amdgcn_fdot2_f32_bf16(__builtin_bit_cast(bf16x2, a), __builtin_bit_cast(bf16x2, b), c, false); }
DI s16x8 cat(s16x4 lo, s16x4 hi) { return __builtin_shufflevector(lo, hi, 0, 1, 2, 3, 4, 5, 6, 7); }
DI f32x4 mfma(s16x8 a, u32x4 b, f32x4 c) { return __builtin_amdgcn_mfma_f32_16x16x32_bf16(__builtin_bit_cast(bf16x8, a), __builtin_bit_cast(bf16x8, b), c, 0, 0, 0); }
DI void mfma_v(f32x4& c, s16x8 a, u32x4 b) { asm volatile("v_mfma_f32_16x16x32_bf16 %0, %1, %2, %0" : "+v"(c) : "v"(a), "v"(b)); }
DI void mfma_v0(f32x4& c, s16x8 a, u32x4 b) { asm volatile("v_mfma_f32_16x16x32_bf16 %0, %1, %2, 0" : "=&v"(c) : "v"(a), "v"(b)); }
DI void mfma_a(f32x4& c, s16x8 a, u32x4 b) { asm volatile("v_mfma_f32_16x16x32_bf16 %0, %1, %2, %0" : "+v"(c) : "v"(a), "a"(b)); }
DI void mfma_a0(f32x4& c, s16x8 a, u32x4 b) { asm volatile("v_mfma_f32_16x16x32_bf16 %0, %1, %2, 0" : "=&v"(c) : "v"(a), "a"(b)); }
DI void mfma_acc(f32x4& c, s16x8 a, u32x4 b) { asm volatile("v_mfma_f32_16x16x32_bf16 %0, %1, %2, %0" : "+a"(c) : "v"(a), "v"(b)); }
DI void mfma_acc_a(f32x4& c, s16x8 a, u32x4 b) { asm volatile("v_mfma_f32_16x16x32_bf16 %0, %1, %2, %0" : "+a"(c) : "a"(a), "v"(b)); }
DI void pin_a(f32x4& a) { asm volatile("" : "+a"(a)); }
DI void mfma_av(f32x4& c, s16x8 a, u32x4 b) { asm volatile("v_mfma_f32_16x16x32_bf16 %0, %1, %2, %0" : "+v"(c) : "a"(a), "v"(b)); }
DI void mfma_av0(f32x4& c, s16x8 a, u32x4 b) { asm volatile("v_mfma_f32_16x16x32_bf16 %0, %1, %2, 0" : "=&v"(c) : "a"(a), "v"(b)); }
DI void mfma_aa(f32x4& c, s16x8 a, u32x4 b) { asm volatile("v_mfma_f32_16x16x32_bf16 %0, %1, %2, %0" : "+v"(c) : "a"(a), "a"(b)); }
DI void mfma_aa0(f32x4& c, s16x8 a, u32x4 b) { asm volatile("v_mfma_f32_16x16x32_bf16 %0, %1, %2, 0" : "=&v"(c) : "a"(a), "a"(b)); }
template <int OFF> DI void trrd_a(s16x4& d, u32 a) { asm volatile("ds_read_b64_tr_b16 %0, %1 offset:%2" : "=a"(d) : "v"(a), "i"(OFF) : "memory"); }
template <int OFF> DI void trrd_v(s16x4& d, u32 a) { asm volatile("ds_read_b64_tr_b16 %0, %1 offset:%2" : "=v"(d) : "v"(a), "i"(OFF) : "memory"); }
template <int OFF> DI void rd128_v(u32x4& d, u32 a) { asm volatile("ds_read_b128 %0, %1 offset:%2" : "=v"(d) : "v"(a), "i"(OFF) : "memory"); }
template <int OFF> DI void ld128_a(u32x4& d, u32 voff, srd_t srd, u32 soff) {
  asm volatile("buffer_load_dwordx4 %0, %1, %2, %3 offen offset:%4 " PAUX : "=a"(d) : "v"(voff), "s"(srd), "s"(soff), "i"(OFF) : "memory"); }
template <int N> DI void wl() { asm volatile("s_waitcnt lgkmcnt(%0)" :: "i"(N) : "memory"); }
DI void zero_v(f32x4& a) { f32x2 lo, hi; asm volatile("v_mov_b64 %0, 0\n\tv_mov_b64 %1, 0" : "=v"(lo), "=v"(hi)); a = __builtin_shufflevector(lo, hi, 0, 1, 2, 3); }
DI float fmax2(float a, float b) { return __builtin_elementwise_maximum(a, b); }
template <int R> struct FR;
#define DEF_FR(R, A0, A1, A2, A3) template <> struct FR<R> { \
  template <int OFF> static DI void lo(s16x4& d, u32 a) { asm volatile("ds_read_b64_tr_b16 %0, %1 offset:%2" : "={a[" #A0 ":" #A1 "]}"(d) : "v"(a), "i"(OFF) : "memory"); } \
  template <int OFF> static DI void hi(s16x4& d, u32 a) { asm volatile("ds_read_b64_tr_b16 %0, %1 offset:%2" : "={a[" #A2 ":" #A3 "]}"(d) : "v"(a), "i"(OFF) : "memory"); } \
  static DI void vv(f32x4& c, s16x4 l, s16x4 h, u32x4 b) { asm volatile("v_mfma_f32_16x16x32_bf16 %0, a[" #A0 ":" #A3 "], %3, %0" : "+v"(c) : "{a[" #A0 ":" #A1 "]}"(l), "{a[" #A2 ":" #A3 "]}"(h), "v"(b)); } \
  static DI void vv0(f32x4& c, s16x4 l, s16x4 h, u32x4 b) { asm volatile("v_mfma_f32_16x16x32_bf16 %0, a[" #A0 ":" #A3 "], %3, 0" : "=&v"(c) : "{a[" #A0 ":" #A1 "]}"(l), "{a[" #A2 ":" #A3 "]}"(h), "v"(b)); } \
  static DI void va(f32x4& c, s16x4 l, s16x4 h, u32x4 b) { asm volatile("v_mfma_f32_16x16x32_bf16 %0, a[" #A0 ":" #A3 "], %3, %0" : "+v"(c) : "{a[" #A0 ":" #A1 "]}"(l), "{a[" #A2 ":" #A3 "]}"(h), "a"(b)); } \
  static DI void va0(f32x4& c, s16x4 l, s16x4 h, u32x4 b) { asm volatile("v_mfma_f32_16x16x32_bf16 %0, a[" #A0 ":" #A3 "], %3, 0" : "=&v"(c) : "{a[" #A0 ":" #A1 "]}"(l), "{a[" #A2 ":" #A3 "]}"(h), "a"(b)); } \
  static DI void k16(f32x4& c, s16x4 l, u32x2 b) { asm volatile("v_mfma_f32_16x16x16_bf16 %0, a[" #A0 ":" #A1 "], %2, %0" : "+v"(c) : "{a[" #A0 ":" #A1 "]}"(l), "v"(b)); } \
  static DI void av(f32x4& c, s16x4 l, s16x4 h, u32x4 b) { asm volatile("v_mfma_f32_16x16x32_bf16 %0, a[" #A0 ":" #A3 "], %3, %0" : "+a"(c) : "{a[" #A0 ":" #A1 "]}"(l), "{a[" #A2 ":" #A3 "]}"(h), "v"(b)); } };
DEF_FR(0, 208, 209, 210, 211)
DEF_FR(1, 212, 213, 214, 215)
DEF_FR(2, 216, 217, 218, 219)
DEF_FR(3, 220, 221, 222, 223)
DEF_FR(4, 224, 225, 226, 227)
DEF_FR(5, 228, 229, 230, 231)
DEF_FR(6, 232, 233, 234, 235)
DEF_FR(7, 236, 237, 238, 239)
DEF_FR(8, 240, 241, 242, 243)
DEF_FR(9, 244, 245, 246, 247)
DEF_FR(10, 248, 249, 250, 251)
DEF_FR(11, 252, 253, 254, 255)
DI void mfma_vn(f32x4& c, s16x8 a, u32x4 b) { asm volatile("s_nop 1\n\tv_mfma_f32_16x16x32_bf16 %0, %1, %2, %0" : "+v"(c) : "v"(a), "v"(b)); }
DI void mfma_v0n(f32x4& c, s16x8 a, u32x4 b) { asm volatile("s_nop 1\n\tv_mfma_f32_16x16x32_bf16 %0, %1, %2, 0" : "=&v"(c) : "v"(a), "v"(b)); }
DI void mfma16_c(f32x4& d, u32x2 a, u32x2 b, f32x4 c) { asm volatile("v_mfma_f32_16x16x16_bf16 %0, %1, %2, %3" : "=&v"(d) : "v"(a), "v"(b), "v"(c)); }
DI void mfma16_v(f32x4& d, u32x2 a, u32x2 b) { asm volatile("v_mfma_f32_16x16x16_bf16 %0, %1, %2, %0" : "+v"(d) : "v"(a), "v"(b)); }
template <int CTRL> DI float dppadd(float v) { return v + __int_as_float(__builtin_amdgcn_update_dpp(0, __float_as_int(v), CTRL, 0xf, 0xf, true)); }
DI u32 v_lshl16(u32 a) { u32 r; asm volatile("v_lshlrev_b32 %0, 16, %1" : "=v"(r) : "v"(a)); return r; }
DI u32 v_andhi(u32 a) { u32 r; asm volatile("v_and_b32 %0, 0xffff0000, %1" : "=v"(r) : "v"(a)); return r; }
DI u32 v_fmav(u32 a, float b, float c) { u32 r; asm volatile("v_fma_f32 %0, %1, %2, %3" : "=v"(r) : "v"(a), "v"(b), "v"(c)); return r; }
DI u32 v_cvtv(u32 a, u32 b) { u32 r; asm volatile("v_cvt_pk_bf16_f32 %0, %1, %2" : "=v"(r) : "v"(a), "v"(b)); return r; }
DI u32 v_cvtf(float a, float b) { u32 r; asm volatile("v_cvt_pk_bf16_f32 %0, %1, %2" : "=v"(r) : "v"(a), "v"(b)); return r; }
DI u32 v_addv(u32 a, u32 s) { u32 r; asm volatile("v_add_u32 %0, %1, %2" : "=v"(r) : "s"(s), "v"(a)); return r; }
DI void v_swap(u32& a, u32& b) { asm volatile("v_swap_b32 %0, %1" : "+v"(a), "+v"(b)); }
DI void keep4(u32x4 a, u32x4 b, u32x4 c, u32x4 d) { asm volatile("" :: "v"(a), "v"(b), "v"(c), "v"(d)); }
DI void pin(f32x4& a) { asm volatile("" : "+v"(a)); }
DI void pin2(float& a, float& b) { asm volatile("" : "+v"(a), "+v"(b)); }
DI void zero_a(f32x4& a) { a = f32x4{0, 0, 0, 0}; asm volatile("" : "+a"(a)); }
DI u32 sopaque(u32 v) { asm volatile("" : "+s"(v)); return v; }
DI float max2(float a, float b) { float r; asm("v_max_f32 %0, %1, %2" : "=v"(r) : "v"(a), "v"(b)); return r; }
DI float max3(float a, float b, float c) { float r; asm("v_max3_f32 %0, %1, %2, %3" : "=v"(r) : "v"(a), "v"(b), "v"(c)); return r; }
DI void pin1(float& a) { asm volatile("" : "+v"(a)); }
DI void pinu(u32x4& a) { asm volatile("" : "+v"(a)); }
DI void pinu2(u32x2& a, u32x2& b) { asm volatile("" : "+v"(a), "+v"(b)); }
DI void undef_v(f32x4& a) { asm volatile("" : "=v"(a)); }
DI void undef_a(u32x4& a) { asm volatile("" : "=a"(a)); }
DI void pinw(u32& a) { asm volatile("" : "+v"(a)); }
DI void wr64_a(u32 a, u32x4 d) {
  asm volatile("ds_write_b64 %0, %1\n\tds_write_b64 %0, %2 offset:%3" :: "v"(a), "a"(__builtin_shufflevector(d, d, 0, 1)), "a"(__builtin_shufflevector(d, d, 2, 3)), "i"(80 * VROW) : "memory"); }
DI void mfma2_v(f32x4& c, s16x4 l, s16x4 h, u32x4 b) { mfma_v(c, cat(l, h), b); }
DI void mfma2_v0(f32x4& c, s16x4 l, s16x4 h, u32x4 b) { mfma_v0(c, cat(l, h), b); }
DI void ld128_vs(u32x4& d, u32 voff, srd_t srd, u32 soff) { asm volatile("buffer_load_dwordx4 %0, %1, %2, %3 offen sc1" : "=v"(d) : "v"(voff), "s"(srd), "s"(soff) : "memory"); }
DI void ld64_v(u32x2& d, u32 voff, srd_t srd, u32 soff) { asm volatile("buffer_load_dwordx2 %0, %1, %2, %3 offen" : "=v"(d) : "v"(voff), "s"(srd), "s"(soff) : "memory"); }
DI void pinw_a(u32& a) { asm volatile("" : "+a"(a)); }
DI u32x2 rd64(u32 a) { return *(LDS3 u32x2*)(size_t)a; }
DI void pins(s16x8& a) { asm volatile("" : "+v"(a)); }
DI void nop8(f32x4 (&a)[4][2]) { asm volatile("s_nop 7" : "+v"(a[0][0]), "+v"(a[0][1]), "+v"(a[1][0]), "+v"(a[1][1]), "+v"(a[2][0]), "+v"(a[2][1]), "+v"(a[3][0]), "+v"(a[3][1])); }
DI srd_t code_srd(int& last) {
  unsigned long long pc; int len;
  asm volatile("s_getpc_b64 %0\n1:\n\ts_mov_b32 %1, fused_code_end-1b" : "=s"(pc), "=s"(len));
  last = len > 2048 ? len - 2048 : 0;
  return mksrd((const void*)pc, len > 2048 ? (u32)len : 0u);
}
template <int OFF> DI void tlb_touch(u32& d, const void* p) { asm volatile("s_load_dword %0, %1, %2" : "+s"(d) : "s"(p), "i"(OFF) : "memory"); }
DI void tlb_done(u32& d) { asm volatile("s_waitcnt lgkmcnt(0)" : "+s"(d) :: "memory"); }
template <int OFF> DI void ld128_v(u32x4& d, u32 voff, srd_t srd, u32 soff) { asm volatile("buffer_load_dwordx4 %0, %1, %2, %3 offen offset:%4 sc1" : "=v"(d) : "v"(voff), "s"(srd), "s"(soff), "i"(OFF) : "memory"); }
DI u32 v_relu(u32 a) { u32 r; asm volatile("v_pk_max_i16 %0, %1, 0" : "=v"(r) : "v"(a)); return r; }
DI u32 rfl(u32 v) { return __builtin_amdgcn_readfirstlane(v); }
DI void st128(u32x4 d, srd_t s, u32 v, u32 so) { if (ABL & 4) v = OOB; if (ABL & 8) { __builtin_amdgcn_raw_buffer_store_b128(d, s, v, so, 0); return; } __builtin_amdgcn_raw_buffer_store_b128(d, s, v, so, SAUX); }
DI u32x4 ld128(srd_t s, u32 v, u32 so) { return __builtin_amdgcn_raw_buffer_load_b128(s, v, so, 16); }
DI void st_flag(u32 val, srd_t s, u32 v0, u32 so) { __builtin_amdgcn_raw_buffer_store_b32(val, s, v0, so, 16); }
DI bool flags_ok(u32 word, u32 tag, u32 need) { const u32 d = word - tag; return __all(need == 0 || (d < 16u && d >= need)); }
DI void spin(u32 voff, srd_t s, u32 so, u32 tag, u32 need) {
#pragma unroll 1
  for (u32 i = 0; i < SPIN_MAX; ++i) { u32 wd; poll_issue(wd, voff, s, so); waitvm_v<0>(wd); if (flags_ok(wd, tag, need)) break; __builtin_amdgcn_s_sleep(1); }
}
DI void spin2(u32 voff, srd_t s, u32 so, u32 lo, u32 lim) {
#pragma unroll 1
  for (u32 i = 0; i < SPIN_MAX; ++i) { __builtin_amdgcn_s_sleep(1); u32 wd; poll_issue(wd, voff, s, so); waitvm_v<0>(wd); if (__all(wd - lo <= lim)) break; }
}
DI void spin2n(u32 voff, srd_t s, u32 so, u32 lo, u32 lim) {
#pragma unroll 1
  for (u32 i = 0; i < SPIN_MAX; ++i) { u32 wd; poll_issue(wd, voff, s, so); waitvm_v<0>(wd); if (__all(wd - lo <= lim)) break; __builtin_amdgcn_s_sleep(1); }
}
#define STAMP(i)
#define STAMP_DECL
#define STAMP_FLUSH()

enum { OP_W = 1, OP_HS, OP_HB, OP_HC, OP_FS, OP_FLAG, OP_POLL, OP_MISC, OP_SL, OP_PL };
constexpr int dpos(int m) { return m == 3 ? 0 : m == 9 ? 1 : m == 15 ? 2 : m == 21 ? 3 : m == 27 ? 4 : m == 30 ? 5 : -1; }
constexpr int fpos(int m) { return (m == 6 || m == 12 || m == 18 || m == 24) ? m / 6 - 1 : (m >= 35 && m % 4 == 3) ? 4 + (m - 35) / 4 : -1; }
constexpr int f2op(bool O, int J, int m) {
  if (J <= 17 && dpos(m) >= 0) return OP_W;
  if (J == -2) return (m == 34 || m == 37 || m == 40 || m == 43 || m == 46) ? OP_MISC : 0;
  if (J == -1) return m == 46 ? OP_MISC : 0;
  const int p = fpos(m);
  auto at = [&](int mask) { return p >= 0 && ((mask >> p) & 1); };
  if (J <= 5 && at(J == 0 ? 0x2E : J == 1 ? 0x2D : 0x25)) return OP_HS;
  if ((J == 2 && m == 47) || (J == 6 && m == 47) || (J == 15 && m == 12) || (J == 17 && m == 10)) return OP_FLAG;
  if ((J == 3 && m == 43) || (J == 7 && m == 47) || (J == 12 && m == 33) || (J == 16 && m == 33) || (J == 18 && m == 44) || (J == 19 && m == 47)) return OP_POLL;
  constexpr int hb[13] = {0, 0, 0, 0, 0xD0, 0x5A, 0x0F, 0x01, 0x50, 0x50, 0x2E, 0x0F, 0}, hc[13] = {0, 0, 0, 0, 0, 0, 0, 0x54, 0x0F, 0xAF, 0xC1, 0xF0, 0x0F};
  if (J <= 12 && at(hb[J])) return OP_HB;
  if (J <= 12 && at(hc[J])) return OP_HC;
  if ((J == 14 || J == 16) && (m == 2 || m == 5 || m == 8 || m == 11)) return OP_FS;
  constexpr int sl[5] = {0x50, 0x5C, 0x5D, 0xAC, 0x0F};
  if ((J >= 13 && J <= 17 && at(sl[J - 13])) || (J == 18 && m < 42 && m % 5 == 1) || (J == 19 && m < 30 && m % 4 == 1)) return OP_SL;
  if ((J == 17 && m >= 33 && m % 2 == 1) || (J == 19 && m >= 32 && m % 2 == 0) || (J == 20 && m >= 26 && m < 34 && m % 2 == 0)) return OP_PL;
  if (J == 20 && m < 16 && m % 4 == 2) return OP_MISC;
  return 0;
}
constexpr int f2count(bool O, int J0, int m0, int J1, int m1) {
  int n = 0;
  for (int J = J0; J <= J1; ++J) for (int m = 0; m < 48; ++m) { if (J == J0 && m <= m0) continue; if (J == J1 && m >= m1) continue; n += f2op(O, J, m) != 0 && f2op(O, J, m) != OP_PL; }
  if (J0 < 0 && J1 >= 0) n += 4;
  return n > 63 ? 63 : n;
}
constexpr int f2index(bool O, int J, int m) {
  int n = 0; const int k = f2op(O, J, m);
  for (int j = 0; j <= J; ++j) for (int q = 0; q < 48; ++q) { if (j == J && q >= m) break; n += f2op(O, j, q) == k; }
  return n;
}
constexpr int f2find(bool O, int k, int idx) {
  int n = 0;
  for (int j = 0; j < 21; ++j) for (int q = 0; q < 48; ++q) if (f2op(O, j, q) == k) { if (n == idx) return 48 * j + q; ++n; }
  return -1;
}
constexpr int f2after(bool O, int k, int idx, int J, int m) { return f2count(O, f2find(O, k, idx) / 48, f2find(O, k, idx) % 48, J, m); }
constexpr int f2after_min(int k, int idx, int J, int m) { return f2after(true, k, idx, J, m); }
constexpr int imin(int a, int b) { return a < b ? a : b; }
static_assert(f2find(1, OP_HS, 19) >= 0 && f2find(1, OP_HS, 20) < 0 && f2find(1, OP_SL, 35) >= 0 && f2find(1, OP_SL, 36) < 0 && f2find(1, OP_PL, 19) >= 0 && f2find(1, OP_PL, 20) < 0 &&
              f2find(1, OP_HB, 23) >= 0 && f2find(1, OP_HB, 24) < 0 && f2find(1, OP_HC, 23) >= 0 && f2find(1, OP_HC, 24) < 0 && f2find(1, OP_FS, 7) >= 0, "schedule");

struct XState { u32 vx[4]; srd_t srd; float rs[2], sh[2]; u32 bx; };
DI void x_dma(const XState& X, u32 ldsdst, u32 soff) {
  setm0(ldsdst);
  sfor<4>([&](auto P) { dma16<P.value * 1024>(X.vx[P.value], X.srd, soff); });
}
DI u32x4 norm_frag(u32x4 r, float rs, float sh) {
  u32x4 o;
#pragma unroll
  for (int i = 0; i < 4; ++i) o[i] = cvtpk(__builtin_fmaf(bflo(r[i]), rs, sh), __builtin_fmaf(bfhi(r[i]), rs, sh));
  return o;
}
template <int K> DI void norm_op(const XState& X, const u32x4 (&raw)[2], u32x4 (&Bn)[2], u32 (&tlo)[2], u32 (&thi)[2]) {
  constexpr int blk = K / 10, r = K % 10, i = 2 * blk + (r & 1), tb = i >> 2, d = i & 3, op = r >> 1, z = r & 1;
  if constexpr (op == 0) tlo[z] = v_lshl16(raw[tb][d]);
  if constexpr (op == 1) thi[z] = v_andhi(raw[tb][d]);
  if constexpr (op == 2) tlo[z] = v_fmav(tlo[z], X.rs[tb], X.sh[tb]);
  if constexpr (op == 3) thi[z] = v_fmav(thi[z], X.rs[tb], X.sh[tb]);
  if constexpr (op == 4) Bn[tb][d] = v_cvtv(tlo[z], thi[z]);
}
template <int N> DI void ln_group(u32x4 (&p)[9], u32 dst, u32 xw) {
  wait9<N>(p);
  wr128(xw, p[0]); wr128(xw + 4096, p[1]);
  float a0 = 0, a1 = 0, b0 = 0, b1 = 0;
#pragma unroll
  for (int c = 0; c < 9; ++c) { a0 = dot2(p[c][0], 0x3f803f80u, a0); a1 = dot2(p[c][1], 0x3f803f80u, a1); a0 = dot2(p[c][2], 0x3f803f80u, a0); a1 = dot2(p[c][3], 0x3f803f80u, a1);
    b0 = dot2(p[c][0], p[c][0], b0); b1 = dot2(p[c][1], p[c][1], b1); b0 = dot2(p[c][2], p[c][2], b0); b1 = dot2(p[c][3], p[c][3], b1); }
  const float a = dppadd<0x141>(dppadd<0x4E>(dppadd<0xB1>(a0 + a1))), b = dppadd<0x141>(dppadd<0x4E>(dppadd<0xB1>(b0 + b1)));
  const float m = a * (1.f / 576.f), var = fmaxf(b * (1.f / 576.f) - m * m, 0.f);
  const float rs = __builtin_amdgcn_rcpf(rbf(__builtin_amdgcn_sqrtf(rbf(rbf(var) + 1e-5f))));
  wr64(dst, u32x2{__float_as_uint(rs), __float_as_uint(-rbf(m) * rs)});
  asm volatile("" ::: "memory");
}
DI void epoch_issue(u32& e, const char* p) { asm volatile("s_load_dword %0, %1, 0x0" : "=s"(e) : "s"(p) : "memory"); }
DI void epoch_wait(u32& e0, u32& e1) { asm volatile("s_waitcnt lgkmcnt(0)" : "+s"(e0), "+s"(e1) :: "memory"); }

__global__ __launch_bounds__(256, 1) __attribute__((flatten)) void fused(const u16* __restrict__ x, const u16* __restrict__ wqkv, const u16* __restrict__ wo, const u16* __restrict__ w1,
                                               const u16* __restrict__ w2, u16* __restrict__ out, char* __restrict__ ws) {
  extern __shared__ __attribute__((aligned(16))) char smem[];
  STAMP_DECL;
  STAMP(0);
  const u32 ldsb = (u32)(size_t)(LDS3 char*)smem;
  const u32 l = threadIdx.x & 63, w = rfl(threadIdx.x >> 6);
  const u32 bid = blockIdx.x, xcd = bid & 7, jj = bid >> 3;
  const u32 t7 = jj & 7, tile = (xcd & 1) * 8 + t7, sq = xcd >> 1, role = jj >> 3;
  const u32 tok0 = tile * 128;
  const u32 r8 = l >> 3, s8 = l & 7;
  const u32 g = l >> 4, tk = l & 15, q4 = (l >> 2) & 3, p4 = l & 3, rr = 4 * (g & 1) + q4, hh = g >> 1;
  {
    int cl; const srd_t cs = code_srd(cl); const u32 co = (jj * 4 + w) * 1024;
    setm0(ldsb + TBASE); dma16<0>(l * 16, cs, co > (u32)cl ? (u32)cl : co); }
  XState X;
  X.srd = mksrd_b(x, SEQ * XROW);
  const u32 pa0 = BIAS + (tok0 + 32 * w + 2 * r8) * XROW + 16 * s8;
  auto grp_so = [&](int i) { const u32 p = (i + XROT * role) & 3; return (16 * (p & 1) + (p >> 1)) * XROW; };
  auto ld_part = [&](u32x4 (&p)[9], int i, auto LO, auto N) { const u32 so = grp_so(i); sfor<N.value>([&](auto C) { constexpr int c = LO.value + C.value; ld128_asm<c * 128>(p[c], pa0, X.srd, so); }); };
  auto ld_group = [&](u32x4 (&p)[9], int i) { ld_part(p, i, IC(0), IC(9)); };
  auto xwr = [&](int i) { const u32 p = (i + XROT * role) & 3; return ldsb + XBASE + w * 8192 + (8 * p + r8) * 128 + 16 * (s8 ^ (r8 >> 1) ^ (4 * (p & 1))); };
  auto sdst = [&](int i) { return ldsb + SBASE + w * 256 + (8 * ((i + XROT * role) & 3) + r8) * 8; };
  u32 ep0, ep1;
  srd_t srdWS;
  u32 v16, v0, xdst, colswz;
  auto common = [&]() {
    epoch_issue(ep0, ws + tile * WS_TILE + WS_EPOCH);
    epoch_issue(ep1, ws + ((tile + 1) & 15) * WS_TILE + WS_EPOCH);
    srdWS = mksrd(ws, WS_END);
    v16 = l * 16; v0 = l == 0 ? 0u : OOB;
#pragma unroll
    for (int p = 0; p < 4; ++p) X.vx[p] = BIAS + (tok0 + 32 * w + 16 * (p & 1) + 2 * r8 + (p >> 1)) * XROW + 16 * (s8 ^ (r8 >> 1) ^ (4 * (p & 1))) - 1024 * p;
#pragma unroll
    for (int p = 0; p < 4; ++p) pinw(X.vx[p]);
    X.bx = ldsb + XBASE + w * 8192 + tk * 128 + 16 * (g ^ (tk >> 1));
    xdst = ldsb + XBASE + w * 8192;
    colswz = 32 * ((s8 >> 1) ^ (r8 >> 1)) + 16 * (s8 & 1);
  };
  auto ln_fetch = [&]() {
    u32x2 t0, t1;
    asm volatile("ds_read_b64 %0, %2\n\tds_read_b64 %1, %2 offset:128\n\ts_waitcnt lgkmcnt(0)" : "=&v"(t0), "=&v"(t1) : "v"(ldsb + SBASE + w * 256 + tk * 8) : "memory");
    X.rs[0] = __uint_as_float(t0[0]); X.sh[0] = __uint_as_float(t0[1]); X.rs[1] = __uint_as_float(t1[0]); X.sh[1] = __uint_as_float(t1[1]); };
  if (__builtin_expect(role < 3, 1)) {
    u32x4 pA[9], pB[9];
    ld_group(pA, 0); ld_part(pB, 1, IC(0), IC(NFIRST - 9));
    SB0();
    common();
    const u32 slice = sq * 3 + role;
    const u32 om = (sq + role) & 3;
    const u32 gm[3] = {om == 0 ? 1u : om == 1 ? 2u : 0u, om == 0 ? 2u : om == 1 ? 0u : 1u, om == 3 ? 2u : om};
    srd_t dsrd = mksrd_b(w1, DM * W1ROW);
    u32 dvl = (8 * (r8 >> 2) + (r8 & 3)) * W1ROW + colswz;
    u32 dk[6];
#pragma unroll
    for (int c = 0; c < 6; ++c) { const u32 pi = 6 * w + c; dk[c] = BIAS + (16 * (pi & 1) + 4 * ((pi >> 1) & 1)) * W1ROW + slice * 768 + (pi >> 2) * 128 - 1024 * (c & 3); }
    u32 dso = 0, dd1 = 32 * W1ROW, dd2 = 32 * W1ROW, dwrap = ~0u;
    const u32 dm0lo = ldsb + w * 6144, dm0hi = dm0lo + FNS * FSLOT;
    u32 dm0 = dm0lo;
    auto dma_piece = [&](auto C) { constexpr int c = C.value; dma16<(c & 3) * 1024>(dvl, dsrd, dso + dk[c]); };
    auto dma_adv = [&](auto E) {
      if constexpr (E.value == 0) dso += dd1; else { dso += dd2; dso = dso == dwrap ? 0 : dso; }
      dm0 += FSLOT; dm0 = dm0 == dm0hi ? dm0lo : dm0; };
    auto dma_all = [&](auto E) { setm0(dm0); sfor<4>(dma_piece); setm0(dm0 + 4096); dma_piece(IC(4)); dma_piece(IC(5)); dma_adv(E); };
    constexpr int NAA = 4;
    f32x4 acc[24][2];
    auto zacc = [&](auto LO, auto N) { sfor<N.value>([&](auto I) { constexpr int k = LO.value + I.value; if constexpr (k >= 2 * NAA) zero_v(acc[k >> 1][k & 1]); else zero_a(acc[k >> 1][k & 1]); }); };
    zacc(IC(0), IC(48));
    u32 cur[4], nxt[4];
    u32 rdelta = FSLOT, rslot = 0;
    constexpr int LA = 8, NF = 12;
    s16x4 Fl[NF], Fh[NF];
    auto rdhalf = [&](auto A, auto T, const u32 (&bs)[4]) { constexpr int a = A.value;
      if constexpr (T.value == 0) FR<a % NF>::template lo<(a >> 2) * 4096>(Fl[a % NF], bs[a & 3]); else FR<a % NF>::template hi<(a >> 2) * 4096 + 2048>(Fh[a % NF], bs[a & 3]); };
#pragma unroll
    for (int u = 0; u < 4; ++u) cur[u] = ldsb + hh * 1024 + 128 * rr + 32 * (u ^ (rr >> 1)) + 8 * p4;
    srd_t psrd = mksrd(w2, DFF * XROW);
    u32 pso_ = (slice * 384 + 48 * t7) * XROW + w * 1024;
    u32 plim_ = (slice * 384 + 48 * t7 + 48) * XROW - 1024;
    ld_part(pB, 1, IC(NFIRST - 9), IC(18 - NFIRST));
    dma_all(IC(0));
    ln_group<15>(pA, sdst(0), xwr(0));
    ld_group(pA, 2);
    ln_group<15>(pB, sdst(1), xwr(1));
    ld_group(pB, 3);
    ln_group<9>(pA, sdst(2), xwr(2));
    dma_all(IC(1));
    __builtin_amdgcn_s_barrier();
    u32x4 B[2], Bn[2], raw[2];
    sfor<LA>([&](auto A) { rdhalf(A, IC(0), cur); rdhalf(A, IC(1), cur); });
    ln_group<6>(pB, sdst(3), xwr(3));
    rd128_v<0>(raw[0], X.bx); rd128_v<2048>(raw[1], X.bx);
    setm0(ldsb + TBASE); dma16<0>(v16, psrd, pso_);
    dma_all(IC(0));
    setm0(ldsb + TBASE); dma16<0>(v16, psrd, pso_);
    STAMP(1);
    ln_fetch();
    epoch_wait(ep0, ep1);
    u32 tl = 0;
    if (TLBW) { tlb_touch<0xF0000>(tl, w1); tlb_touch<0xF0000>(tl, (const char*)w1 + 0x100000); tlb_touch<0xF0000>(tl, (const char*)w1 + 0x200000); tlb_touch<0xF0000>(tl, (const char*)w1 + 0x300000); tlb_touch<0xF0000>(tl, (const char*)w1 + 0x400000);
      tlb_touch<0>(tl, ws + tile * WS_TILE + WS_TILE - 256); tlb_touch<0>(tl, (const char*)out + tok0 * XROW); tlb_touch<127 * XROW>(tl, (const char*)out + tok0 * XROW); }
    pinu(raw[0]); pinu(raw[1]);
    B[0] = norm_frag(raw[0], X.rs[0], X.sh[0]); B[1] = norm_frag(raw[1], X.rs[1], X.sh[1]);
    asm volatile("s_nop 1" : "+v"(B[0]), "+v"(B[1]));
    STAMP(2);
    auto stage = [&](auto E, auto NV, auto DMA, auto L8, auto&& MM, auto&& MEM, auto&& EX, auto&& BAR) {
      sfor<48>([&](auto M) { constexpr int m = M.value, a = m >> 1, tb = m & 1;
        if constexpr (m == 32 && NV.value >= 0) { if constexpr (NV.value < 63) waitvm<NV.value>(); __builtin_amdgcn_s_barrier(); BAR(); }
        if constexpr (m % 8 == 0) wl<(NV.value < 0 && m >= 40) ? 0 : (m == 8 ? L8.value : 8)>();
        MM(M);
        if constexpr (a + LA < 24) rdhalf(IC(a + LA), IC(tb), cur); else if constexpr (NV.value >= 0) rdhalf(IC(a + LA - 24), IC(tb), nxt);
        if constexpr (DMA.value && dpos(m) >= 0) dma_piece(IC(dpos(m)));
        MEM(M);
        SB0();
        if constexpr (DMA.value && m == 2) setm0_add<0>(dm0);
        if constexpr (DMA.value && m == 26) setm0_add<4096>(dm0);
        if constexpr (DMA.value && m == 31) dma_adv(IC(E.value ^ 1));
        if constexpr (m == 1) { rslot += FSLOT; rdelta = rslot == FNS * FSLOT ? 0u - (FNS - 1) * FSLOT : FSLOT; rslot = rslot == FNS * FSLOT ? 0 : rslot; }
        if constexpr (m >= 2 && m < 6 && NV.value >= 0) nxt[m - 2] = v_addv(cur[m - 2], rdelta);
        EX(M);
        SB0();
      });
#pragma unroll
      for (int u = 0; u < 4; ++u) cur[u] = nxt[u];
    };
    auto nop = [&](auto) {};
    auto nop0 = [&]() {};
    constexpr auto tbo = [](int m) { return GRAY ? ((m ^ (m >> 1)) & 1) : (m & 1); };
    u32 xs = 0, xso = 256, ba = X.bx ^ 64;
    u32 tlo[2], thi[2];
    for (int it = 0; it < 9; ++it) {
      sfor<2>([&](auto E) { constexpr int e = E.value;
        if constexpr (e == 0) { if (it == 7) { X.srd = mksrd(x, 0); int last; psrd = code_srd(last); plim_ = (u32)last; pso_ = (t7 & 1) * 16384 + w * 1024; pso_ = pso_ > plim_ ? plim_ : pso_; } }
        if constexpr (e == 1) { if (it == 7) {
            dsrd = mksrd_b(w2, DFF * XROW); dvl = r8 * XROW + colswz;
#pragma unroll
            for (int c = 0; c < 6; ++c) dk[c] = BIAS + (48 * w + 8 * c) * XROW + role * 384 - 1024 * (c & 3);
            dso = slice * 384 * XROW + gm[0] * 128; dd1 = 192 * XROW; dd2 = (gm[1] - gm[0]) * 128 - 192 * XROW;
          } }
        stage(E, IC(e == 0 ? 14 : 18), IC(1), IC(6),
              [&](auto M) { constexpr int a = M.value >> 1, tb = tbo(M.value); if constexpr (a >= NAA) FR<a % NF>::vv(acc[a][tb], Fl[a % NF], Fh[a % NF], B[tb]); else FR<a % NF>::av(acc[a][tb], Fl[a % NF], Fh[a % NF], B[tb]); },
              [&](auto M) { constexpr int m = M.value;
                if constexpr (m == 0) rd128_v<0>(raw[0], ba);
                if constexpr (m == 1) rd128_v<2048>(raw[1], ba);
                if constexpr (e == 0 && m >= 34 && m < 46 && (m - 34) % 3 == 0) dma16<((m - 34) / 3) * 1024>(X.vx[(m - 34) / 3], X.srd, xso);
                if constexpr (m == 46) dma16<0>(v16, psrd, pso_); },
              [&](auto M) { constexpr int m = M.value;
                if constexpr (e == 0 && m == 33) setm0_add<0>(xdst + xs);
                if constexpr (e == 0 && m == 44) { xso += 128; xs ^= 4096; }
                if constexpr (m == 45) setm0_add<TBASE>(ldsb);
                if constexpr (m == 47) { pso_ += 4096; pso_ = pso_ > plim_ ? plim_ : pso_; }
                if constexpr (m == 6) ba = v_addv(e == 0 ? X.bx : (X.bx ^ 64), e == 0 ? (xs ^ 4096) : xs);
                if constexpr (m >= 8) norm_op<m - 8>(X, raw, Bn, tlo, thi); },
              nop0);
        B[0] = Bn[0]; B[1] = Bn[1];
        asm volatile("s_nop 0" : "+v"(B[0]), "+v"(B[1]));
      });
    }
    STAMP(3);
    tlb_done(tl);
    asm volatile("s_nop 7");
    u32x4 H[12][2];
    const u32 hb0 = tile * WS_TILE + WS_H + (slice * 4 + w) * 24576;
#pragma unroll
    for (int k = 0; k < 2; ++k)
#pragma unroll
      for (int tb = 0; tb < 2; ++tb) {
        pin_a(acc[2 * k][tb]); pin_a(acc[2 * k + 1][tb]);
#pragma unroll
        for (int i = 0; i < 4; ++i) { const f32x4 s = acc[2 * k + (i >> 1)][tb];
          H[k][tb][i] = __builtin_bit_cast(u32, __builtin_elementwise_max(__builtin_bit_cast(s16x2, cvtpk(s[2 * (i & 1)], s[2 * (i & 1) + 1])), s16x2{0, 0})); }
        st128(H[k][tb], srdWS, v16 + (2 * k + tb) * 1024, hb0);
      }
    u32 ct[2];
    auto conv_op = [&](auto N) { constexpr int n = N.value;
      if constexpr (n >= 1 && n <= 80) { constexpr int q = n - 1, k = 2 + (q >> 3), tb = (q >> 2) & 1, i = q & 3; H[k][tb][i] = v_relu(ct[q & 1]); }
      if constexpr (n < 80) { constexpr int k = 2 + (n >> 3), tb = (n >> 2) & 1, i = n & 3; ct[n & 1] = v_cvtf(acc[2 * k + (i >> 1)][tb][2 * (i & 1)], acc[2 * k + (i >> 1)][tb][2 * (i & 1) + 1]); } };
    const u32 tag = (ep0 + 1) << 4;
    const u32 fso = tile * WS_TILE + WS_FLAG + w * 128;
    const u32 slB = sq * 3 + (role == 2 ? 0 : role + 1), slC = sq * 3 + (role == 0 ? 2 : role - 1);
    const u32 sbase[3] = {slice * 384 * XROW, slB * 384 * XROW, slC * 384 * XROW};
    const u32 hbB = tile * WS_TILE + WS_H + (slB * 4 + w) * 24576, hbC = tile * WS_TILE + WS_H + (slC * 4 + w) * 24576;
    const u32 pg0 = tile * WS_TILE + WS_P + (role * 48 + w) * 4096;
    const u32 vpoll = l < 16 ? l * 4 : OOB;
    const u32 lim12 = (l == slB || l == slC) ? 13u : ~0u;
    const bool owner = om < 3;
    const u32 pown = pg0 + gm[2] * 65536;
    const u32 sqi[3] = {(om + 2 >= 3 ? om - 1 : om + 2) - role & 3, (om + 1 >= 3 ? om - 2 : om + 1) - role & 3, (3 - role) & 3};
    const u32 ned[3] = {3, 4, 3 + om};
    bool got[3] = {false, false, false};
    f32x4 ac3[3][4][2], acs[4][2];
    u32x4 HB[12][2], HC[12][2], sd[4], OF[18][2], PQ[3][4];
    u32 pw;
    bool f0 = false, f1 = false;
    const u32 pvo = owner ? v16 : OOB, limo = (owner && l >= 12 && l < 16) ? 14u : ~0u, obase = tile * WS_TILE + WS_O + w * 36864;
    s16x8 sel0, sel1;
    auto rdy = [&](auto I) { constexpr int i = I.value; return owner && !got[i] && __all(l != sqi[i] * 3 + role || pw - (tag + ned[i]) <= 15u - ned[i]); };
    auto red_f = [&](u32x4 (&P)[4]) { sfor<4>([&](auto U) { mfma_a(ac3[2][U.value][0], sel0, P[U.value]); mfma_a(ac3[2][U.value][1], sel1, P[U.value]); }); };
    auto gather = [&](auto N) {
#pragma unroll 1
      for (u32 it = 0; it < SPIN_MAX && !(got[0] && got[1] && (N.value < 3 || got[2])); ++it) {
        poll_issue(pw, vpoll, srdWS, fso);
        waitvm_v<0>(pw);
        sfor<3>([&](auto I) { constexpr int i = I.value;
          if (rdy(I)) { got[i] = true; sfor<4>([&](auto U) { ld128_a<U.value * 1024>(PQ[i][U.value], v16, srdWS, pown + sqi[i] * 16384); }); } });
        if (got[0] && got[1] && (N.value < 3 || got[2])) waitvm<0>();
      } };
    auto wo_dk = [&](auto I, auto C) { constexpr int i = I.value, c = C.value;
      const u32 pi = 6 * w + c, k4 = pi >> 2, t = (pi >> 1) & 1, kap = 6 * i + k4;
      u32 gb = 2 * kap + (kap >> 2) + t;
      if constexpr (i == 2) { const u32 alt = (k4 & 1) * 18 + 8 + t * 9; gb = k4 < 4 ? gb : alt; }
      dk[c] = sopaque(BIAS + (16 * gb + 8 * (pi & 1)) * XROW - 1024 * (c & 3)); };
    const srd_t srdX = mksrd_b(x, SEQ * XROW), srdO = mksrd_b(out, SEQ * XROW);
    const u32 ecol = (role * 3 + gm[2]) * 128;
    sfor<12>([&](auto U) { undef_a(PQ[U.value >> 2][U.value & 3]); });
    asm volatile("s_nop 1");
    STAMP(4);
    auto g2stage = [&](auto OO, auto JJ) { constexpr int J = JJ.value; constexpr bool O = OO.value;
      constexpr auto whf = [](int j) { return j < 6 ? 0 : j < 9 ? 1 : j < 12 ? 2 : 1 + (j & 1); };
      constexpr auto gaf = [](int j) { return j < 6 ? j >> 1 : j < 12 ? 0 : (j - 12) >> 1; };
      constexpr auto haf = [](int j) { return j < 6 ? j & 1 : j < 12 ? 0 : 1; };
      constexpr int which = J < 18 ? whf(J) : 3, half = haf(J), Jn = J + 4;
      constexpr int nw = J <= 19 ? f2count(O, J - 2, 30, J, 32) : -1;
      constexpr int nv = J == 4 ? imin(nw, f2after(O, OP_POLL, 0, J, 32)) : J == 8 ? imin(nw, f2after(O, OP_POLL, 1, J, 32)) : J == 13 ? imin(nw, f2after(O, OP_POLL, 2, J, 32)) :
                         J == 17 ? imin(nw, f2after(O, OP_POLL, 3, J, 32)) : nw;
      stage(IC(J & 1), IC(nv), IC(J <= 17), IC(8),
            [&](auto M) { constexpr int m = M.value, a = m >> 1, tb = tbo(m), u = a & 3, ks = a >> 2;
              constexpr bool km = J >= 6 && J < 12;
              constexpr int kap = km ? 2 * (J % 3) + (ks & 1) : 6 * half + ks, gam = km ? ks >> 1 : gaf(J);
              if constexpr ((m == 0 || m == 8) && km) waitvm<f2after(O, J < 9 ? OP_HB : OP_HC, 2 * kap + 1, J, m)>();
              if constexpr (m % 8 == 0 && (J == 12 || J == 13)) waitvm<f2after(O, J == 12 ? OP_HB : OP_HC, 2 * kap + 1, J, m)>();
              if constexpr (m % 8 == 0 && J >= 18) waitvm<f2after(O, OP_SL, 12 * (J - 18) + 2 * ks + 1, J, m)>();
              if constexpr (which == 0) { if constexpr (half == 0 && ks == 0) FR<a % NF>::vv0(ac3[gam][u][tb], Fl[a % NF], Fh[a % NF], H[0][tb]); else FR<a % NF>::vv(ac3[gam][u][tb], Fl[a % NF], Fh[a % NF], H[kap][tb]); }
              else if constexpr (which == 1) FR<a % NF>::va(ac3[gam][u][tb], Fl[a % NF], Fh[a % NF], HB[kap][tb]);
              else if constexpr (which == 2) FR<a % NF>::va(ac3[gam][u][tb], Fl[a % NF], Fh[a % NF], HC[kap][tb]);
              else if constexpr (J == 18 && ks == 0) FR<a % NF>::vv0(acs[u][tb], Fl[a % NF], Fh[a % NF], OF[0][tb]);
              else if constexpr (J < 20) FR<a % NF>::vv(acs[u][tb], Fl[a % NF], Fh[a % NF], OF[6 * (J - 18) + ks][tb]);
              else FR<a % NF>::va(acs[u][tb], Fl[a % NF], Fh[a % NF], OF[12 + ks][tb]); },
            [&](auto M) { constexpr int m = M.value, op = f2op(O, J, m), i = f2index(O, J, m);
              if constexpr (op == OP_HS) st128(H[2 + (i >> 1)][i & 1], srdWS, v16 + (i & 3) * 1024, hb0 + 4096 + (i >> 2) * 4096);
              if constexpr (op == OP_HB) ld128_a<(i & 3) * 1024>(HB[i >> 1][i & 1], v16, srdWS, hbB + (i >> 2) * 4096);
              if constexpr (op == OP_HC) ld128_a<(i & 3) * 1024>(HC[i >> 1][i & 1], v16, srdWS, hbC + (i >> 2) * 4096);
              if constexpr (op == OP_FS) st128(sd[i & 3], srdWS, v16 + (i & 3) * 1024, pg0 + gm[i >> 2] * 65536 + sq * 16384);
              if constexpr (op == OP_SL) { if constexpr (i < 24) ld128_v<(i & 3) * 1024>(OF[i >> 1][i & 1], pvo, srdWS, obase + (i >> 2) * 4096); else ld128_a<(i & 3) * 1024>(OF[i >> 1][i & 1], pvo, srdWS, obase + (i >> 2) * 4096); }
              if constexpr (op == OP_PL) { constexpr int q = (i >= 16 || (i & 4)) ? 2 : (i >> 3);
                if ((i >= 16 || (i & 4)) ? f1 : f0) ld128_a<(i & 3) * 1024>(PQ[q][i & 3], v16, srdWS, pown + sqi[q] * 16384); }
              if constexpr (op == OP_MISC) dma16<i * 1024>(X.vx[i], srdX, ecol);
              if constexpr (op == OP_POLL) poll_issue(pw, vpoll, srdWS, fso);
              if constexpr (op == OP_FLAG) {
                waitvm<(i == 0 ? f2after(O, OP_HS, 7, J, m) : i == 1 ? f2after(O, OP_HS, 19, J, m) : f2after(O, OP_FS, 4 * (i - 2) + 3, J, m))>();
                st_flag(tag + 1 + i, srdWS, v0, fso + slice * 4); }
              if constexpr ((J == 14 || J == 16) && m == 13) keep4(sd[0], sd[1], sd[2], sd[3]); },
            [&](auto M) { constexpr int m = M.value;
              if constexpr (J < 2) conv_op(IC(48 * J + m));
              if constexpr (m == 40 && Jn < 18) dso = (Jn >= 6 && Jn < 12) ? sbase[whf(Jn)] + (Jn % 3) * 64 * XROW : sbase[whf(Jn)] + haf(Jn) * 192 * XROW + gm[gaf(Jn)] * 128;
              if constexpr ((J == 2 || J == 8) && m >= 41 && m < 47) { constexpr int c = m - 41; const u32 pi = 6 * w + c;
                dk[c] = sopaque(J == 2 ? BIAS + (32 * ((pi >> 2) & 1) + 8 * (pi & 3)) * XROW + role * 384 + ((pi >> 3) == 0 ? gm[0] : (pi >> 3) == 1 ? gm[1] : gm[2]) * 128 - 1024 * (c & 3)
                                       : BIAS + 8 * pi * XROW + role * 384 - 1024 * (c & 3)); }
              if constexpr ((J == 14 || J == 16) && m < 8) { constexpr int u = m >> 1, tb = m & 1, gp = (J - 14) >> 1;
                sd[u][2 * tb] = v_cvtf(ac3[gp][u][tb][0], ac3[gp][u][tb][1]); sd[u][2 * tb + 1] = v_cvtf(ac3[gp][u][tb][2], ac3[gp][u][tb][3]); }
              if constexpr (J == 18 && m == 44) {
                const u32 on = (tk >> 2) == g ? 0x3f80u << (16 * (tk & 1)) : 0u, s0 = (tk & 2) ? 0u : on, s1 = (tk & 2) ? on : 0u;
                sel0 = __builtin_bit_cast(s16x8, u32x4{s0, s1, 0u, 0u}); sel1 = __builtin_bit_cast(s16x8, u32x4{0u, 0u, s0, s1}); pins(sel0); pins(sel1); }
              if constexpr (J >= 14 && J <= 16 && m >= 41 && m < 47) wo_dk(IC(J - 14), IC(m - 41));
              if constexpr (J == 14 && m == 47) { dsrd = owner ? mksrd_b(wo, DM * XROW) : mksrd(wo, 0); dso = ecol; dd1 = dd2 = 0; }
              if constexpr (J == 20 && m == 1) setm0_add<0>(xdst);
              if constexpr (J == 19 && m == 30) { waitvm<f2after_min(OP_POLL, 4, J, 31)>(); waitvm_v<63>(pw); f0 = rdy(IC(1)); f1 = rdy(IC(2)); got[1] |= f0; got[2] |= f1; }
              if constexpr (J == 20 && m == 24) { waitvm<f2after_min(OP_POLL, 5, J, 25)>(); waitvm_v<63>(pw); f1 = rdy(IC(2)); got[2] |= f1; }
              if constexpr (J == 20 && m == 34) { gather(IC(2)); red_f(PQ[0]); red_f(PQ[1]); } },
            [&]() {
              if constexpr (J == 4) { waitvm_v<63>(pw); if (!__all(pw - (tag + 1) <= lim12 + (lim12 != ~0u))) spin2(vpoll, srdWS, fso, tag + 1, lim12 + (lim12 != ~0u)); }
              if constexpr (J == 8) { waitvm_v<63>(pw); if (!__all(pw - (tag + 2) <= lim12)) spin2(vpoll, srdWS, fso, tag + 2, lim12); }
              if constexpr (J == 13) { waitvm_v<63>(pw); if (!__all(pw - (tag + 1) <= limo)) spin2(vpoll, srdWS, fso, tag + 1, limo); }
              if constexpr (J == 17) { waitvm_v<63>(pw); f0 = rdy(IC(0)); f1 = rdy(IC(2)); got[0] |= f0; got[2] |= f1; } });
    };
    sfor<18>([&](auto JJ) { g2stage(std::true_type{}, JJ); if constexpr (JJ.value == 5) STAMP(5); });
    STAMP(6);
    nop8(ac3[2]);
    if (!owner) {
      sfor<8>([&](auto I) { constexpr int u = I.value >> 1, tb = I.value & 1;
        sd[u][2 * tb] = v_cvtf(ac3[2][u][tb][0], ac3[2][u][tb][1]); sd[u][2 * tb + 1] = v_cvtf(ac3[2][u][tb][2], ac3[2][u][tb][3]); });
      asm volatile("s_nop 0");
      sfor<4>([&](auto U) { st128(sd[U.value], srdWS, v16 + U.value * 1024, pown + sq * 16384); });
      waitvm<0>();
      st_flag(tag + 5, srdWS, v0, fso + slice * 4);
      STAMP(7);
      STAMP_FLUSH();
      return;
    }
    sfor<3>([&](auto JJ) { g2stage(std::true_type{}, IC(18 + JJ.value)); });
    gather(IC(3));
    waitvm<0>();
    red_f(PQ[2]);
    asm volatile("s_nop 7");
    const u32 ia0 = xdst + tk * 128 + 16 * (hh ^ (tk >> 1)) + 8 * (g & 1);
#pragma unroll
    for (int u = 0; u < 4; ++u)
#pragma unroll
      for (int tb = 0; tb < 2; ++tb) {
        pin(ac3[2][u][tb]); pin(acs[u][tb]);
        const u32 ia = (ia0 ^ (32 * u)) + tb * 2048;
        const u32x2 xv = rd64(ia);
        u32x2 o;
#pragma unroll
        for (int d = 0; d < 2; ++d) {
          const f32x2 f = {ac3[2][u][tb][2 * d], ac3[2][u][tb][2 * d + 1]}, sv = {acs[u][tb][2 * d], acs[u][tb][2 * d + 1]};
          const f32x2 t = f * -1.4426950408889634f;
          const f32x2 e = f32x2{__builtin_amdgcn_exp2f(t[0]), __builtin_amdgcn_exp2f(t[1])} + 1.f;
          const f32x2 gt = f * f32x2{__builtin_amdgcn_rcpf(e[0]), __builtin_amdgcn_rcpf(e[1])};
          const f32x2 r = __builtin_elementwise_fma(sv, gt, f32x2{bflo(xv[d]), bfhi(xv[d])});
          o[d] = cvtpk(r[0], r[1]);
        }
        wr64(ia, o);
      }
#pragma unroll
    for (int c = 0; c < 4; ++c) __builtin_amdgcn_raw_buffer_store_b128(rd128(xdst + c * 1024 + v16), srdO, X.vx[c] + c * 1024, ecol, 0);
    if (role == 0 && om == 0 && w == 0) st_flag(ep0 + 1, srdWS, v0, tile * WS_TILE + WS_EPOCH);
    STAMP(7);
    STAMP_FLUSH();
  } else {
    u32x4 pA[9], pB[9];
    ld_group(pA, 0); ld_part(pB, 1, IC(0), IC(NFIRST - 9));
    SB0();
    common();
    srd_t dsrd = mksrd_b(wqkv, DM * WQROW);
    u32 dv[7];
#pragma unroll
    for (int c = 0; c < 7; ++c) { u32 pi = 7 * w + c; pi = pi > 26 ? 26 : pi;
      const u32 seg = pi / 9, idx = (pi - seg * 9) * 64 + l, rho = idx / 18, ch = idx - rho * 18;
      const u32 krow = 16 * ((rho >> 3) & 1) + 4 * (rho >> 4) + 8 * ((rho >> 2) & 1) + (rho & 3);
      dv[c] = BIAS + krow * WQROW + seg * 1152 + sq * 288 + ch * 16 - 1024 * (c & 3); }
    u32 dso = 0;
    const u32 dm0lo = ldsb + w * 7168, dm0hi = dm0lo + ANS * ASLOT;
    u32 dm0 = dm0lo;
    auto dma_piece = [&](auto C) { constexpr int c = C.value; dma16<(c & 3) * 1024>(dv[c], dsrd, dso); };
    auto dma_adv = [&]() { dso += 32 * WQROW; dm0 += ASLOT; dm0 = dm0 == dm0hi ? dm0lo : dm0; };
    auto dma_all = [&]() { setm0(dm0); sfor<4>(dma_piece); setm0(dm0 + 4096); dma_piece(IC(4)); dma_piece(IC(5)); dma_piece(IC(6)); dma_adv(); };
    f32x4 acc[27][2];
    auto zacc = [&](auto LO, auto N) { sfor<N.value>([&](auto I) { constexpr int k = LO.value + I.value; if constexpr (k < 18) zero_a(acc[k >> 1][k & 1]); else zero_v(acc[k >> 1][k & 1]); }); };
    zacc(IC(0), IC(54));
    u32 cur = ldsb + (8 * hh + rr) * 288 + 8 * p4, nxt = cur, rslot = 0, rdelta = ASLOT;
    constexpr int LA = 6;
    s16x4 Fl[9], Fh[9];
    auto rdhalf = [&](auto A, auto T, u32 bs) { constexpr int a = A.value, off = (a / 9) * 9216 + (a % 9) * 32;
      if constexpr (T.value == 0) FR<a % 9>::template lo<off>(Fl[a % 9], bs); else FR<a % 9>::template hi<off + 16 * 288>(Fh[a % 9], bs); };
    const srd_t psrd = mksrd(wqkv, DM * WQROW);
    const u32 pf0 = l < 48 ? (4 * t7 + l / 12) * WQROW + ((sq * 288) & ~127u) + (l % 12) * 32 : OOB;
    u32 pfl = w < 3 ? pf0 + w * 1152 : l * 32;
    srd_t psrdl = w < 3 ? psrd : mksrd(wo, DM * XROW);
    const int plo_ = w < 3 ? 0 : (144 * sq + 18 * t7) * XROW;
    int phi_ = w < 3 ? 17 * 32 * WQROW : plo_ + 18 * XROW - 2048, pst_ = w < 3 ? 32 * WQROW : 2048;
    int pso_ = w < 3 ? 6 * 32 * WQROW : plo_ - 7 * 2048;
    ld_part(pB, 1, IC(NFIRST - 9), IC(18 - NFIRST));
    dma_all();
    ln_group<16>(pA, sdst(0), xwr(0));
    ld_group(pA, 2);
    ln_group<16>(pB, sdst(1), xwr(1));
    ld_group(pB, 3);
    ln_group<9>(pA, sdst(2), xwr(2));
    dma_all();
    __builtin_amdgcn_s_barrier();
    u32x4 B[2], Bn[2], raw[2];
    sfor<LA>([&](auto A) { rdhalf(A, IC(0), cur); rdhalf(A, IC(1), cur); });
    ln_group<7>(pB, sdst(3), xwr(3));
    rd128_v<0>(raw[0], X.bx); rd128_v<2048>(raw[1], X.bx);
    setm0(ldsb + TBASE); sfor<3>([&](auto S) { touch(pf0, psrd, (2 + w) * 32 * WQROW + S.value * 1152); });
    STAMP(1);
    ln_fetch();
    epoch_wait(ep0, ep1);
    u32 tl = 0;
    if (TLBW) { tlb_touch<0xF0000>(tl, wqkv); tlb_touch<0xE0000>(tl, (const char*)wqkv + 0x100000); tlb_touch<0>(tl, wo); tlb_touch<0xA0000>(tl, wo); }
    pinu(raw[0]); pinu(raw[1]);
    B[0] = norm_frag(raw[0], X.rs[0], X.sh[0]); B[1] = norm_frag(raw[1], X.rs[1], X.sh[1]);
    asm volatile("s_nop 1" : "+v"(B[0]), "+v"(B[1]));
    STAMP(2);
    u32 xs = 0, xso = 256, ba = X.bx ^ 64;
    u32 tlo[2], thi[2];
    for (int it = 0; it < 9; ++it) {
      if (it == 6 && w < 3) { psrdl = code_srd(phi_); pfl = l * 32; pso_ = (int)w * 2048 > phi_ ? phi_ : (int)w * 2048; pst_ = 6144; }
      if (it == 7) X.srd = mksrd(x, 0);
      if (it == 8) dsrd = mksrd(wqkv, 0);
      sfor<2>([&](auto E) { constexpr int e = E.value;
        sfor<54>([&](auto M) { constexpr int m = M.value, a = m >> 1, tb = m & 1, tq = GRAY ? ((m ^ (m >> 1)) & 1) : tb;
          if constexpr (m == 42) { waitvm<e == 0 ? 8 : 12>(); __builtin_amdgcn_s_barrier(); }
          if constexpr (m % 6 == 0) wl<6>();
          if constexpr (a < 9) FR<a % 9>::av(acc[a][tq], Fl[a % 9], Fh[a % 9], B[tq]); else FR<a % 9>::vv(acc[a][tq], Fl[a % 9], Fh[a % 9], B[tq]);
          if constexpr (a + LA < 27) rdhalf(IC(a + LA), IC(tb), cur); else rdhalf(IC(a + LA - 27), IC(tb), nxt);
          if constexpr (m % 6 == 3 && m < 42) dma_piece(IC(m / 6));
          if constexpr (m == 0) rd128_v<0>(raw[0], ba);
          if constexpr (m == 1) rd128_v<2048>(raw[1], ba);
          if constexpr (e == 0 && m >= 44 && m < 52 && (m & 1) == 0) dma16<((m - 44) / 2) * 1024>(X.vx[(m - 44) / 2], X.srd, xso);
          if constexpr (m == 52) touch(pfl, psrdl, (u32)(pso_ < plo_ ? plo_ : pso_));
          SB0();
          if constexpr (m == 2) setm0_add<0>(dm0);
          if constexpr (m == 26) setm0_add<4096>(dm0);
          if constexpr (m == 40) dma_adv();
          if constexpr (e == 0 && m == 43) setm0_add<0>(xdst + xs);
          if constexpr (e == 0 && m == 53) { xso += 128; xs ^= 4096; }
          if constexpr (m == 51) setm0_add<TBASE>(ldsb);
          if constexpr (m == 53) { pso_ += pst_; pso_ = pso_ > phi_ ? phi_ : pso_; }
          if constexpr (m == 1) { rslot += ASLOT; rdelta = rslot == ANS * ASLOT ? 0u - (ANS - 1) * ASLOT : ASLOT; rslot = rslot == ANS * ASLOT ? 0 : rslot; }
          if constexpr (m == 4) nxt = v_addv(cur, rdelta);
          if constexpr (m == 6) ba = v_addv(e == 0 ? X.bx : (X.bx ^ 64), e == 0 ? (xs ^ 4096) : xs);
          if constexpr (m >= 12 && m < 52) norm_op<m - 12>(X, raw, Bn, tlo, thi);
          SB0();
        });
        cur = nxt; B[0] = Bn[0]; B[1] = Bn[1];
      });
    }
    STAMP(3);
    tlb_done(tl);
    asm volatile("s_nop 7\n\ts_waitcnt vmcnt(0) lgkmcnt(0)" ::: "memory");
    u32x4 Kp[9], Kq[9], Vp[9];
    u32x2 Qp[9][2];
    const bool hsend = w == 3 && tile < 15;
    const u32 hsn = (tile + 1) * WS_TILE + WS_HALO + sq * 18432, hfn = (tile + 1) * WS_TILE + WS_HFLAG + sq * 128;
#pragma unroll
    for (int b = 0; b < 9; ++b) { pin(acc[9 + b][0]); pin(acc[9 + b][1]);
      Kp[b] = u32x4{cvtpk(acc[9 + b][0][0], acc[9 + b][0][1]), cvtpk(acc[9 + b][0][2], acc[9 + b][0][3]), cvtpk(acc[9 + b][1][0], acc[9 + b][1][1]), cvtpk(acc[9 + b][1][2], acc[9 + b][1][3])}; }
    if (hsend) {
#pragma unroll
      for (int b = 0; b < 9; ++b) st128(Kp[b], srdWS, v16, hsn + b * 1024);
    }
#pragma unroll
    for (int b = 0; b < 9; ++b) { pin(acc[18 + b][0]); pin(acc[18 + b][1]);
      Vp[b] = u32x4{cvtpk(acc[18 + b][0][0], acc[18 + b][0][1]), cvtpk(acc[18 + b][0][2], acc[18 + b][0][3]), cvtpk(acc[18 + b][1][0], acc[18 + b][1][1]), cvtpk(acc[18 + b][1][2], acc[18 + b][1][3])}; }
    __builtin_amdgcn_s_barrier();
    const u32 vw = ldsb + (16 * (w + 1) + tk) * VROW + 8 * g;
    auto wr_v = [&]() {
#pragma unroll
      for (int b = 0; b < 9; ++b) { wr64(vw + 32 * b, u32x2{Vp[b][0], Vp[b][1]}); wr64(vw + 80 * VROW + 32 * b, u32x2{Vp[b][2], Vp[b][3]}); } };
    if (w < 3) {
      wr_v();
#pragma unroll
      for (int b = 0; b < 9; ++b) wr128(ldsb + XBASE + w * 9216 + b * 1024 + v16, Kp[b]);
      waitlgkm<0>();
    } else if (hsend) {
      waitvm<0>(); st_flag(((ep1 + 1) << 4) + 1, srdWS, v0, hfn);
#pragma unroll
      for (int b = 0; b < 9; ++b) st128(Vp[b], srdWS, v16, hsn + 9216 + b * 1024);
    }
    __builtin_amdgcn_s_barrier();
    if (w == 3) wr_v();
#pragma unroll
    for (int b = 0; b < 9; ++b)
#pragma unroll
      for (int tb = 0; tb < 2; ++tb) { pin_a(acc[b][tb]); Qp[b][tb] = u32x2{cvtpk(acc[b][tb][0], acc[b][tb][1]), cvtpk(acc[b][tb][2], acc[b][tb][3])}; }
#pragma unroll
    for (int b = 0; b < 9; ++b) pinu2(Qp[b][0], Qp[b][1]);
    u32x4 Vh[9];
    const u32 hv = tile > 0 ? v16 : OOB;
    const u32 hso = tile * WS_TILE + WS_HALO + sq * 18432, hfo = tile * WS_TILE + WS_HFLAG + sq * 128;
    if (hsend) { waitvm<0>(); st_flag(((ep1 + 1) << 4) + 2, srdWS, v0, hfn); }
    if (w > 0) {
#pragma unroll
      for (int b = 0; b < 9; ++b) Kq[b] = rd128(ldsb + XBASE + (w - 1) * 9216 + b * 1024 + v16);
    } else {
      if (tile > 0) spin(v0, srdWS, hfo, (ep0 + 1) << 4, l == 0 ? 1u : 0u);
#pragma unroll
      for (int b = 0; b < 9; ++b) ld128_vs(Kq[b], hv, srdWS, hso + b * 1024);
    }
    wait9<0>(Kq);
    STAMP(4);
    f32x4 cmc, cmp; u32 mcur[2];
    {
      const bool hasprev = tile > 0 || w > 0;
      u32 mb[4];
#pragma unroll
      for (int r = 0; r < 4; ++r) { const int i = 4 * g + r, q = tk; const bool vc = i <= q && i + 7 >= q, vp = hasprev && i >= q + 9;
        cmc[r] = vc ? 0.f : -1e30f; cmp[r] = vp ? 0.f : -1e30f; mb[r] = vc ? 0xffffu : 0u; }
      mcur[0] = mb[0] | (mb[1] << 16); mcur[1] = mb[2] | (mb[3] << 16);
    }
    s16x8 ones = {0x3f80, 0x3f80, 0x3f80, 0x3f80, 0x3f80, 0x3f80, 0x3f80, 0x3f80};
    const float sc = 0.23570226039551584f * 1.4426950408889634f;
    const u32 vr = ldsb + (16 * w + 4 * g + q4) * VROW + 8 * p4;
    f32x4 Sc[2][8], Sp[2][8], Ol[2][8], Oh[2][8], Sm[2][8];
    u32x4 Pf[2][8];
    s16x8 Vt[2][9];
    u32x2 qh[8], ql[8], Ob[2][9];
    auto qmask = [&](auto TB) { constexpr int tb = TB.value;
#pragma unroll
      for (int h = 0; h < 8; ++h) {
#pragma unroll
        for (int d = 0; d < 2; ++d) { qh[h][d] = (2 * g + d >= (u32)h) ? Qp[h][tb][d] : 0u; ql[h][d] = (2 * g + d <= (u32)h) ? Qp[h + 1][tb][d] : 0u; }
        pinu2(qh[h], ql[h]); }
      asm volatile("s_nop 1"); };
    auto phA = [&](auto TB, auto HP, auto J) { constexpr int tb = TB.value, j = J.value, h = 2 * HP.value + ((j >> 1) & 1);
      if constexpr (j < 4) { if constexpr (j & 1) mfma16_c(Sp[tb][h], u32x2{Kq[h][2 * tb], Kq[h][2 * tb + 1]}, qh[h], cmp); else mfma16_c(Sc[tb][h], u32x2{Kp[h][2 * tb], Kp[h][2 * tb + 1]}, qh[h], cmc); }
      else { if constexpr (j & 1) mfma16_v(Sp[tb][h], u32x2{Kq[h + 1][2 * tb], Kq[h + 1][2 * tb + 1]}, ql[h]); else mfma16_v(Sc[tb][h], u32x2{Kp[h + 1][2 * tb], Kp[h + 1][2 * tb + 1]}, ql[h]); } };
    f32x4 val[2]; float mx[2];
    auto phB = [&](auto TB, auto HP, auto I) { constexpr int tb = TB.value, i = I.value;
#pragma unroll
      for (int e = 0; e < 2; ++e) { const int h = 2 * HP.value + e;
        if constexpr (i == 0) { pin(Sc[tb][h]); pin(Sp[tb][h]);
#pragma unroll
          for (int r = 0; r < 4; ++r) val[e][r] = fmax2(Sc[tb][h][r], Sp[tb][h][r]);
          mx[e] = fmax2(fmax2(val[e][0], val[e][1]), fmax2(val[e][2], val[e][3])); }
        if constexpr (i == 1) {
          { auto s = __builtin_amdgcn_permlane16_swap(__float_as_uint(mx[e]), __float_as_uint(mx[e]), false, false); mx[e] = fmax2(__uint_as_float(s[0]), __uint_as_float(s[1])); }
          { auto s = __builtin_amdgcn_permlane32_swap(__float_as_uint(mx[e]), __float_as_uint(mx[e]), false, false); mx[e] = fmax2(__uint_as_float(s[0]), __uint_as_float(s[1])); }
          mx[e] = -mx[e] * sc; }
        if constexpr (i == 2) {
#pragma unroll
          for (int r = 0; r < 4; ++r) val[e][r] = __builtin_amdgcn_exp2f(__builtin_fmaf(val[e][r], sc, mx[e])); }
        if constexpr (i == 3) { const u32 k0 = cvtpk(val[e][0], val[e][1]), k1 = cvtpk(val[e][2], val[e][3]);
          Pf[tb][h] = u32x4{k0 & ~mcur[0], k1 & ~mcur[1], k0 & mcur[0], k1 & mcur[1]}; } }
      if constexpr (i < 2) pin2(mx[0], mx[1]);
      if constexpr (i == 2) { pin(val[0]); pin(val[1]); }
      if constexpr (i == 3) { pinu(Pf[tb][2 * HP.value]); pinu(Pf[tb][2 * HP.value + 1]); } };
    auto phC = [&](auto TB, auto HP, auto J) { constexpr int tb = TB.value, j = J.value, h = 2 * HP.value + j / 3, i = j % 3;
      if constexpr (j < 6) {
        if constexpr (i == 0) mfma_v0(Sm[tb][h], ones, Pf[tb][h]);
        if constexpr (i == 1) mfma_v0(Ol[tb][h], Vt[tb][h], Pf[tb][h]);
        if constexpr (i == 2) mfma_v0(Oh[tb][h], Vt[tb][h + 1], Pf[tb][h]); } };
    auto phD = [&](auto TB, auto HD) { constexpr int tb = TB.value, h = HD.value;
      pin(Sm[tb][h]); pin(Ol[tb][h]); pin(Oh[tb][h]);
      const float inv = __builtin_amdgcn_rcpf(Sm[tb][h][0]);
#pragma unroll
      for (int d = 0; d < 2; ++d) { const u32 lo = cvtpk(Ol[tb][h][2 * d] * inv, Ol[tb][h][2 * d + 1] * inv);
        if (h == 0) Ob[tb][0][d] = lo; else Ob[tb][h][d] = (2 * g + d < (u32)h) ? Ob[tb][h][d] : lo;
        Ob[tb][h + 1][d] = cvtpk(Oh[tb][h][2 * d] * inv, Oh[tb][h][2 * d + 1] * inv); }
      pinu2(Ob[tb][h], Ob[tb][h + 1]); };
    auto ld_vt = [&](auto TB) { constexpr int tb = TB.value;
      asm volatile("" ::: "memory");
#pragma unroll
      for (int b = 0; b < 9; ++b) Vt[tb][b] = cat(trrd(vr + tb * 80 * VROW + 32 * b), trrd(vr + (tb * 80 + 16) * VROW + 32 * b)); };
    asm volatile("" : "+v"(cmc), "+v"(cmp), "+v"(ones));
    qmask(IC(0));
    sfor<32>([&](auto J) { phA(IC(0), IC(J.value >> 3), IC(J.value & 7)); });
    qmask(IC(1));
#pragma unroll
    for (int b = 0; b < 9; ++b) undef_a(Vh[b]);
    if (w == 0) {
      if (tile > 0) spin(v0, srdWS, hfo, (ep0 + 1) << 4, l == 0 ? 2u : 0u);
#pragma unroll
      for (int b = 0; b < 9; ++b) ld128_a<0>(Vh[b], hv, srdWS, hso + 9216 + b * 1024);
    }
    asm volatile("s_nop 7");
    sfor<16>([&](auto J) { constexpr int hp = J.value >> 2, i = J.value & 3; phA(IC(1), IC(hp), IC(2 * i)); phA(IC(1), IC(hp), IC(2 * i + 1)); phB(IC(0), IC(hp), IC(i)); });
    if (w == 0) {
      waitvm<0>();
#pragma unroll
      for (int b = 0; b < 9; ++b) { wr64_a(vw - 16 * VROW + 32 * b, Vh[b]); }
    }
    ld_vt(IC(0));
    asm volatile("s_nop 7" :: "v"(cmc), "v"(cmp));
    sfor<16>([&](auto J) { constexpr int hp = J.value >> 2, i = J.value & 3; phC(IC(0), IC(hp), IC(2 * i)); phC(IC(0), IC(hp), IC(2 * i + 1)); phB(IC(1), IC(hp), IC(i)); });
    ld_vt(IC(1));
    asm volatile("s_nop 7");
    sfor<8>([&](auto J) { constexpr int h = J.value; phC(IC(1), IC(h >> 1), IC(3 * (h & 1))); phC(IC(1), IC(h >> 1), IC(3 * (h & 1) + 1)); phC(IC(1), IC(h >> 1), IC(3 * (h & 1) + 2)); phD(IC(0), J); });
    const u32 obase = tile * WS_TILE + WS_O + w * 36864;
    auto st_o = [&](auto TB) { constexpr int tb = TB.value;
#pragma unroll
      for (int j = 0; j < 4; ++j) st128(u32x4{Ob[tb][2 * j][0], Ob[tb][2 * j][1], Ob[tb][2 * j + 1][0], Ob[tb][2 * j + 1][1]}, srdWS, v16 + tb * 1024, obase + (4 * sq + j) * 2048);
      __builtin_amdgcn_raw_buffer_store_b64(Ob[tb][8], srdWS, v16 + tb * 1024 + 8 * (sq & 1), obase + (16 + (sq >> 1)) * 2048, 16); };
    st_o(IC(0));
    asm volatile("s_nop 7");
    sfor<8>([&](auto J) { phD(IC(1), J); });
    st_o(IC(1));
    STAMP(5);
    waitvm<0>();
    st_flag(((ep0 + 1) << 4) + 1, srdWS, v0, tile * WS_TILE + WS_FLAG + w * 128 + (12 + sq) * 4);
    STAMP(6); STAMP(7);
    STAMP_FLUSH();
    asm volatile("fused_code_end:");
  }
}

extern "C" void kernel_launch(void* const* d_in, const int* in_sizes, int n_in, void* d_out, int out_size, void* d_ws, size_t ws_size, hipStream_t stream) {
  (void)hipFuncSetAttribute((const void*)fused, hipFuncAttributeMaxDynamicSharedMemorySize, LDS_BYTES);
  fused<<<256, 256, LDS_BYTES, stream>>>((const u16*)d_in[0], (const u16*)d_in[1], (const u16*)d_in[2], (const u16*)d_in[4], (const u16*)d_in[5], (u16*)d_out, (char*)d_ws);
}
```
